```python
import torch

def _softmax(x, axis=-1):
    return torch.softmax(x, dim=axis)
def _gelu(x):
    return torch.nn.functional.gelu(x, approximate="tanh")
def _silu(x):
    return torch.nn.functional.silu(x)
def _sigmoid(x):
    return torch.sigmoid(x)
def _layernorm(x, axis=-1, eps=1e-5):
    m = x.mean(dim=axis, keepdim=True)
    v = x.var(dim=axis, keepdim=True, unbiased=False)
    return (x - m) / torch.sqrt(v + eps)
def _scatter_add(tgt, idx, src, axis=0):
    return tgt.clone().index_add_(axis, idx, src)
def _segment_sum(src, seg, n):
    out = torch.zeros((n,) + src.shape[1:], dtype=src.dtype)
    return out.index_add_(0, seg, src)
def _pad(x, pw):
    flat = tuple(v for pair in reversed(pw) for v in pair)
    return torch.nn.functional.pad(x, flat)

kernel_name = 'parallel_sum__sliding_local_dilated__geglu_ffn__prompt'

def setup_inputs():

    _w = (lambda a: (lambda t: t.to(torch.bfloat16) if t.is_floating_point() else t)(torch.as_tensor(a)))
    def _A_setup():
        B, S, D = 1, 2048, 640
        return {
            "x": _w((lambda *s: torch.randn(*s, dtype=torch.bfloat16) * 0.05)(B, S, D)),
            "wqkv": _w((lambda *s: torch.randn(*s, dtype=torch.bfloat16) * 0.05)(D, 3 * D)),
            "wo": _w((lambda *s: torch.randn(*s, dtype=torch.bfloat16) * 0.05)(D, D)),
        }
    _A_in = _A_setup()

    def _B_setup():
        B, S, D, F = 1, 2048, 640, 1728
        return {
            "x": _w((lambda *s: torch.randn(*s, dtype=torch.bfloat16) * 0.05)(B, S, D)),
            "w_gate": _w((lambda *s: torch.randn(*s, dtype=torch.bfloat16) * 0.05)(D, F)),
            "w_up": _w((lambda *s: torch.randn(*s, dtype=torch.bfloat16) * 0.05)(D, F)),
            "w_down": _w((lambda *s: torch.randn(*s, dtype=torch.bfloat16) * 0.05)(F, D)),
        }
    _B_in = _B_setup()

    return {
        "A_x": _A_in["x"],
        "A_wqkv": _A_in["wqkv"],
        "A_wo": _A_in["wo"],
        "B_x": _B_in["x"],
        "B_w_gate": _B_in["w_gate"],
        "B_w_up": _B_in["w_up"],
        "B_w_down": _B_in["w_down"],
    }

def reference(A_x, A_wqkv, A_wo, B_x, B_w_gate, B_w_up, B_w_down):
    def _A_ref(x, wqkv, wo):
        B, S, D = x.shape
        H = 32; Dh = D // H
        W, dil = 8, 2
        qkv = (lambda x, *d: x.permute(*d))((x @ wqkv).reshape(B, S, 3, H, Dh), 2, 0, 3, 1, 4)
        q, k, v = qkv[0], qkv[1], qkv[2]
        scale = Dh ** -0.5
        pos = torch.arange(S)
        off = torch.arange(W) * dil
        idx = pos[:, None] - off[None, :]
        mask = idx >= 0
        idx_c = torch.where(mask, idx, 0).reshape(-1)
        kg = k[:, :, idx_c, :].reshape(B, H, S, W, Dh)
        vg = v[:, :, idx_c, :].reshape(B, H, S, W, Dh)
        logits = torch.einsum("bhsd,bhswd->bhsw", q, kg) * scale
        logits = torch.where(mask[None, None, :, :], logits, -1e9)
        attn = _softmax(logits, axis=-1)
        o = torch.einsum("bhsw,bhswd->bhsd", attn, vg)
        return (lambda x, *d: x.permute(*d))(o, 0, 2, 1, 3).reshape(B, S, D) @ wo

    def _B_ref(x, w_gate, w_up, w_down):
        return (_gelu(x @ w_gate) * (x @ w_up)) @ w_down

    x = A_x
    xn = _layernorm(x, axis=-1)
    return x + _A_ref(xn, A_wqkv, A_wo) + _B_ref(xn, B_w_gate, B_w_up, B_w_down)
```

```cpp
#include <hip/hip_runtime.h>
#include <hip/hip_bf16.h>
#include <stdint.h>

typedef __attribute__((ext_vector_type(4))) short s16x4;
typedef __attribute__((ext_vector_type(8))) short s16x8;
typedef __attribute__((ext_vector_type(2))) __bf16 bf2;
typedef __attribute__((ext_vector_type(8))) __bf16 bf8;
typedef __attribute__((ext_vector_type(4))) float f32x4;
typedef __attribute__((ext_vector_type(4))) unsigned u32x4;
typedef __attribute__((ext_vector_type(2))) unsigned u32x2;
#define LDS_AS __attribute__((address_space(3)))
#define DEVI __device__ __forceinline__

constexpr int S_ = 2048, D_ = 640, F_ = 1728;
constexpr int KS2 = 74;
constexpr size_t A2_BYTES = (size_t)32 * KS2 * 4096;
constexpr int STAT_OFF = 147456;
constexpr int LDS1_BYTES = STAT_OFF + 3072;
constexpr int QRS = 488;

static __device__ const u32x4 g_ones = {0x3F803F80u, 0x3F803F80u, 0x3F803F80u, 0x3F803F80u};

template <int ROLE> struct Cfg;
template <> struct Cfg<0> {
  static constexpr int WM = 2, WN = 4, MT = 4, NA = 3, NE = 2, WROW = 768, WSTAGE = 24576, XTILE = 16384,
                       XOFF = 98304, WI = 3, XE = 1, XO = 1, ST = 1, ROT = 1;
};
template <> struct Cfg<1> {
  static constexpr int WM = 4, WN = 2, MT = 3, NA = 2, NE = 4, WROW = 512, WSTAGE = 16384, XTILE = 24576,
                       XOFF = 65536, WI = 2, XE = 2, XO = 1, ST = 2, ROT = 2;
};

DEVI void dma16(const char* g, unsigned lds_off) {
  asm volatile("s_mov_b32 m0, %0\n\ts_nop 0\n\tglobal_load_lds_dwordx4 %1, off" ::"s"(lds_off), "v"(g) : "memory");
}
template <int N> DEVI void wait_vm() { asm volatile("s_waitcnt vmcnt(%0)" ::"n"(N) : "memory"); }
DEVI void wait_lgkm0() { asm volatile("s_waitcnt lgkmcnt(0)" ::: "memory"); }
DEVI bf8 tr_frag(unsigned a0, unsigned a1) {
  s16x4 lo = __builtin_amdgcn_ds_read_tr16_b64_v4i16((LDS_AS s16x4*)(uintptr_t)a0);
  s16x4 hi = __builtin_amdgcn_ds_read_tr16_b64_v4i16((LDS_AS s16x4*)(uintptr_t)a1);
  return __builtin_bit_cast(bf8, __builtin_shufflevector(lo, hi, 0, 1, 2, 3, 4, 5, 6, 7));
}
DEVI bf8 lds_b128(unsigned a) { return __builtin_bit_cast(bf8, *(LDS_AS u32x4*)(uintptr_t)a); }
DEVI unsigned pk2(float a, float b) {
  bf2 v = {(__bf16)a, (__bf16)b};
  return __builtin_bit_cast(unsigned, v);
}
DEVI float dot2(unsigned a, unsigned b, float c) {
  return __builtin_amdgcn_fdot2_f32_bf16(__builtin_bit_cast(bf2, a), __builtin_bit_cast(bf2, b), c, false);
}
DEVI unsigned a2_off(int r, int k) {
  return (unsigned)(((r >> 6) * KS2 + (k >> 5)) * 4096 + ((r >> 4) & 3) * 1024 + ((((k >> 3) & 3) * 16 + (r & 15)) * 16) +
                    ((k >> 2) & 1) * 8);
}
template <int ROLE> constexpr int n_issue(int t) {
  return (t <= 15 ? Cfg<ROLE>::WI : 0) + (t <= 14 ? (((t + 5) & 1) ? Cfg<ROLE>::XO : Cfg<ROLE>::XE) : 0);
}

template <int ROLE>
DEVI void phase1_body(const char* __restrict__ x, const char* __restrict__ wa, const char* __restrict__ wb,
                      const char* __restrict__ wo, const char* __restrict__ wd, char* __restrict__ A2,
                      char* __restrict__ B2, int rb, int cg, unsigned lds0, int job) {
  using C = Cfg<ROLE>;
  constexpr int MT = C::MT, NA = C::NA, NE = C::NE, NT = NA * NE;
  const int tid = threadIdx.x, lane = tid & 63;
  const int w = __builtin_amdgcn_readfirstlane(tid >> 6);
  const int g = lane >> 4, m = lane & 15, q = m >> 2, p = lane & 3;
  const int wm = w / C::WN, wn = w % C::WN;
  const int rbase = ROLE == 0 ? rb * 127 - 1 : rb * 176 - 16;

  u32x2 jl[8];
  const bool has_job = (w < 3) && (job + w < 740);
  int jks = 0, jcb = 0;
  if (has_job) {
    const int J = job + w;
    jks = J / 10; jcb = J % 10;
    const char* src = (jks < 20 ? wo + (size_t)(jks * 32) * 1280 : wd + (size_t)(jks * 32 - 640) * 1280) +
                      (size_t)(8 * g) * 1280 + jcb * 128 + m * 8;
#pragma unroll
    for (int j = 0; j < 8; ++j) jl[j] = *(const u32x2*)(src + j * 1280);
  }

  const char* wp[C::WI];
  constexpr int WSTRIDE = ROLE == 0 ? F_ * 2 : 3 * D_ * 2;
#pragma unroll
  for (int i = 0; i < C::WI; ++i) {
    constexpr int CPR = C::WROW / 16;
    const int idx = i * 64 + lane;
    const int row = 4 * w + idx / CPR, pc = idx % CPR;
    const int f = (row & 3) | ((row >> 1) & 4);
    const int u = (pc >> 1) ^ f, hlf = pc & 1;
    if (ROLE == 0) {
      const int a = u >> 3, b = u & 7;
      const char* base = (b & 1) ? wb : wa;
      wp[i] = base + (size_t)row * WSTRIDE + (cg * 192 + a * 64 + (b >> 1) * 16 + hlf * 8) * 2;
    } else {
      int c = 2 * u + hlf;
      c = c > 29 ? 29 : c;
      const int part = c / 10, cc = c % 10;
      const int poff = part == 0 ? 640 : (part == 1 ? 1280 : 0);
      wp[i] = wa + (size_t)row * WSTRIDE + (poff + cg * 80 + cc * 8) * 2;
    }
  }
  constexpr int XI = C::XE + C::XO;
  const char* xp[XI];
  int xstep0 = 128;
#pragma unroll
  for (int i = 0; i < XI; ++i) {
    const int lr = (i * 8 + w) * 8 + (lane >> 3);
    int gr = rbase + lr;
    gr = gr < 0 ? 0 : (gr > S_ - 1 ? S_ - 1 : gr);
    const int ch = (lane & 7) ^ ((lr >> 1) & 7);
    xp[i] = x + (size_t)gr * 1280 + ch * 16;
    if (i == 0 && lr == 0) { xp[i] = (const char*)&g_ones; xstep0 = 0; }
  }
  const unsigned wdst = lds0 + w * (C::WI * 1024);
  const unsigned xdst = lds0 + C::XOFF + w * 1024;
  auto issue_w = [&](int buf) {
#pragma unroll
    for (int i = 0; i < C::WI; ++i) { dma16(wp[i], wdst + buf * C::WSTAGE + i * 1024); wp[i] += 32 * WSTRIDE; }
  };
  auto issue_x = [&](int odd, int buf) {
    if (!odd) {
#pragma unroll
      for (int i = 0; i < C::XE; ++i) { dma16(xp[i], xdst + buf * C::XTILE + i * 8192); xp[i] += (i == 0 ? xstep0 : 128); }
    } else {
#pragma unroll
      for (int i = C::XE; i < XI; ++i) { dma16(xp[i], xdst + buf * C::XTILE + i * 8192); xp[i] += 128; }
    }
  };
  issue_x(0, 0);
  issue_w(0); issue_x(1, 0);
  issue_w(1); issue_x(0, 1);
  issue_w(2); issue_x(1, 1);
  issue_w(3); issue_x(0, 2);

  if (has_job) {
    const int n0 = jcb * 64 + 4 * m, nt = n0 >> 4;
    char* dst = B2 + (size_t)(((nt / 5) * KS2 + jks) * 5 + nt % 5) * 1024 + (g * 16 + (n0 & 15)) * 16;
#pragma unroll
    for (int i = 0; i < 4; ++i) {
      u32x4 o;
#pragma unroll
      for (int j = 0; j < 4; ++j)
        o[j] = __builtin_amdgcn_perm(jl[2 * j + 1][i >> 1], jl[2 * j][i >> 1], (i & 1) ? 0x07060302u : 0x05040100u);
      *(u32x4*)(dst + i * 16) = o;
    }
  }

  unsigned wad[NE], xad[MT];
  {
    const int f = q | ((g & 1) << 2);
#pragma unroll
    for (int e = 0; e < NE; ++e) wad[e] = lds0 + (8 * g + q) * C::WROW + (((wn * NE + e) ^ f) * 32) + p * 8;
#pragma unroll
    for (int j = 0; j < MT; ++j) {
      const int act = (j + wn * C::ROT) % MT;
      const int lr = (wm * MT + act) * 16 + m;
      xad[j] = lds0 + C::XOFF + lr * 128 + ((g ^ ((lr >> 1) & 7)) * 16);
    }
  }
  f32x4 acc[MT][NT];
#pragma unroll
  for (int j = 0; j < MT; ++j)
#pragma unroll
    for (int n = 0; n < NT; ++n) acc[j][n] = (f32x4){0.f, 0.f, 0.f, 0.f};
  float ssum[C::ST], ssq[C::ST];
#pragma unroll
  for (int j = 0; j < C::ST; ++j) { ssum[j] = 0.f; ssq[j] = 0.f; }
  bf8 xf[2][MT], wf[2][NT];

  auto read_frags = [&](int par, unsigned woff, unsigned xoff) {
#pragma unroll
    for (int j = 0; j < MT; ++j) xf[par][j] = lds_b128((xad[j] ^ (par ? 64u : 0u)) + xoff);
#pragma unroll
    for (int a = 0; a < NA; ++a)
#pragma unroll
      for (int e = 0; e < NE; ++e) {
        const unsigned ad = wad[e] + woff;
        wf[par][a * NE + e] = tr_frag(ad + a * 256, ad + a * 256 + 4 * C::WROW);
      }
  };
  auto compute = [&](int par) {
#pragma unroll
    for (int n = 0; n < NT; ++n)
#pragma unroll
      for (int j = 0; j < MT; ++j)
        acc[j][n] = __builtin_amdgcn_mfma_f32_16x16x32_bf16(wf[par][n], xf[par][j], acc[j][n], 0, 0, 0);
#pragma unroll
    for (int j = 0; j < C::ST; ++j) {
      const u32x4 v = __builtin_bit_cast(u32x4, xf[par][j]);
#pragma unroll
      for (int d = 0; d < 4; ++d) { ssum[j] = dot2(v[d], 0x3F803F80u, ssum[j]); ssq[j] = dot2(v[d], v[d], ssq[j]); }
    }
  };

  wait_vm<n_issue<ROLE>(-3) + n_issue<ROLE>(-2) + n_issue<ROLE>(-1)>();
  __builtin_amdgcn_s_barrier();
  read_frags(0, 0, 0);

  int xr = 0;
#define ITER(T, PAR, VM, DOW, DOX, DORD)                                                   \
  {                                                                                         \
    if (DORD) { wait_vm<VM>(); wait_lgkm0(); __builtin_amdgcn_s_barrier(); }                \
    const int xn = xr == 2 ? 0 : xr + 1;                                                    \
    const int xq = xn == 2 ? 0 : xn + 1;                                                    \
    if (DOW) issue_w((T) & 3);                                                              \
    if (DOX) issue_x(PAR ? 0 : 1, PAR ? xr : xq);                                           \
    if (DORD) read_frags(PAR ^ 1, (((T) + 1) & 3) * C::WSTAGE, (PAR ? xn : xr) * C::XTILE); \
    compute(PAR);                                                                           \
    if (PAR) xr = xn;                                                                       \
  }
  constexpr int VMF = n_issue<ROLE>(0) + n_issue<ROLE>(1);
  for (int t = 0; t < 14; t += 2) {
    ITER(t, 0, VMF, true, true, true)
    ITER(t + 1, 1, VMF, true, true, true)
  }
  ITER(14, 0, VMF, true, true, true)
  ITER(15, 1, VMF, true, false, true)
  ITER(16, 0, n_issue<ROLE>(14) + n_issue<ROLE>(15), false, false, true)
  ITER(17, 1, n_issue<ROLE>(15), false, false, true)
  ITER(18, 0, 0, false, false, true)
  ITER(19, 1, 0, false, false, false)
#undef ITER

  LDS_AS float* rsum = (LDS_AS float*)(uintptr_t)(lds0 + STAT_OFF);
  LDS_AS float* rsq = rsum + 192;
  LDS_AS float* csum = rsum + 384;
#pragma unroll
  for (int j = 0; j < C::ST; ++j) {
    float a = ssum[j], b = ssq[j];
    a += __shfl_xor(a, 16); b += __shfl_xor(b, 16);
    a += __shfl_xor(a, 32); b += __shfl_xor(b, 32);
    const int lr = (wm * MT + (j + wn * C::ROT) % MT) * 16 + m;
    if (g == 0) { rsum[lr] = a; rsq[lr] = b; }
  }
  if (wm == 0) {
#pragma unroll
    for (int j = 0; j < MT; ++j) {
      if ((j + wn * C::ROT) % MT == 0 && m == 0) {
#pragma unroll
        for (int a = 0; a < NA; ++a)
#pragma unroll
          for (int e = 0; e < NE; ++e)
            *(LDS_AS f32x4*)(csum + (8 * a + wn * NE + e) * 16 + 4 * g) = acc[j][a * NE + e];
      }
    }
  }
  __syncthreads();
  float ra[MT], rbv[MT];
#pragma unroll
  for (int j = 0; j < MT; ++j) {
    const int lr = (wm * MT + (j + wn * C::ROT) % MT) * 16 + m;
    const float mean = rsum[lr] * (1.f / D_);
    const float var = fmaxf(rsq[lr] * (1.f / D_) - mean * mean, 0.f);
    ra[j] = __builtin_amdgcn_rsqf(var + 1e-5f);
    rbv[j] = -mean * ra[j];
  }
  if (ROLE == 0) {
#pragma unroll
    for (int a = 0; a < NA; ++a) {
      const f32x4 cg4 = *(LDS_AS f32x4*)(csum + (8 * a + wn * 2) * 16 + 4 * g);
      const f32x4 cu4 = *(LDS_AS f32x4*)(csum + (8 * a + wn * 2 + 1) * 16 + 4 * g);
      const int k = 640 + cg * 192 + a * 64 + wn * 16 + 4 * g;
#pragma unroll
      for (int j = 0; j < MT; ++j) {
        const int lr = (wm * MT + (j + wn * C::ROT) % MT) * 16 + m;
        const int gr = rbase + lr;
        float h[4];
#pragma unroll
        for (int r = 0; r < 4; ++r) {
          const float gv = ra[j] * acc[j][a * 2][r] + rbv[j] * cg4[r];
          const float uv = ra[j] * acc[j][a * 2 + 1][r] + rbv[j] * cu4[r];
          const float z = gv * (-2.3022082f - 0.10294324f * gv * gv);
          const float e = __builtin_amdgcn_exp2f(z);
          h[r] = gv * uv * __builtin_amdgcn_rcpf(1.f + e);
        }
        if (lr >= 1 && gr < S_) *(u32x2*)(A2 + a2_off(gr, k)) = (u32x2){pk2(h[0], h[1]), pk2(h[2], h[3])};
      }
    }
  } else {
#pragma unroll
    for (int a = 0; a < NA; ++a)
#pragma unroll
      for (int e = 0; e < NE; ++e) {
        const int tau = 8 * a + wn * NE + e;
        const f32x4 c4 = *(LDS_AS f32x4*)(csum + tau * 16 + 4 * g);
        if (tau < 15) {
#pragma unroll
          for (int j = 0; j < MT; ++j) {
            const int lr = (wm * MT + (j + wn * C::ROT) % MT) * 16 + m;
            const f32x4 v = acc[j][a * NE + e];
            *(LDS_AS u32x2*)(uintptr_t)(lds0 + lr * QRS + tau * 32 + g * 8) =
                (u32x2){pk2(ra[j] * v[0] + rbv[j] * c4[0], ra[j] * v[1] + rbv[j] * c4[1]),
                        pk2(ra[j] * v[2] + rbv[j] * c4[2], ra[j] * v[3] + rbv[j] * c4[3])};
          }
        }
      }
    __syncthreads();
    const float sc = 0.22360679775f * 1.44269504089f;
    for (int it = w; it < 44; it += 8) {
      const int mt = 1 + (it >> 2), h = it & 3;
      const unsigned rq = lds0 + (mt * 16 + m) * QRS + h * 40 + g * 16;
      u32x2 k0a = *(LDS_AS u32x2*)(uintptr_t)(rq - 16 * QRS), k0b = *(LDS_AS u32x2*)(uintptr_t)(rq - 16 * QRS + 8);
      u32x2 k1a = *(LDS_AS u32x2*)(uintptr_t)(rq), k1b = *(LDS_AS u32x2*)(uintptr_t)(rq + 8);
      u32x2 qa = *(LDS_AS u32x2*)(uintptr_t)(rq + 320), qb = *(LDS_AS u32x2*)(uintptr_t)(rq + 328);
      const unsigned va = lds0 + ((mt - 1) * 16 + 4 * g + q) * QRS + 160 + h * 40 + p * 8;
      const bf8 v0 = tr_frag(va, va + 16 * QRS);
      const bf8 v1 = tr_frag(va + 32, va + 16 * QRS + 32);
      if (g == 3) qa = (u32x2){0u, 0u};
      if (g >= 2) qb = (u32x2){0u, 0u};
      const bf8 qf = __builtin_bit_cast(bf8, (u32x4){qa[0], qa[1], qb[0], qb[1]});
      const f32x4 z4 = {0.f, 0.f, 0.f, 0.f};
      const f32x4 s0 = __builtin_amdgcn_mfma_f32_16x16x32_bf16(__builtin_bit_cast(bf8, (u32x4){k0a[0], k0a[1], k0b[0], k0b[1]}), qf, z4, 0, 0, 0);
      const f32x4 s1 = __builtin_amdgcn_mfma_f32_16x16x32_bf16(__builtin_bit_cast(bf8, (u32x4){k1a[0], k1a[1], k1b[0], k1b[1]}), qf, z4, 0, 0, 0);
      const bool prev_ok = (rbase + (mt - 1) * 16) >= 0;
      float l[8];
      float mx = -3.0e38f;
#pragma unroll
      for (int r = 0; r < 4; ++r) {
        const int i = 4 * g + r;
        const bool par = ((m - i) & 1) == 0;
        const bool ok0 = par && (i >= m + 2) && prev_ok;
        const bool ok1 = par && (i <= m);
        l[r] = ok0 ? s0[r] * sc : -3.0e38f;
        l[4 + r] = ok1 ? s1[r] * sc : -3.0e38f;
        mx = fmaxf(mx, fmaxf(l[r], l[4 + r]));
      }
      mx = fmaxf(mx, __shfl_xor(mx, 16));
      mx = fmaxf(mx, __shfl_xor(mx, 32));
      float sum = 0.f;
#pragma unroll
      for (int r = 0; r < 8; ++r) { l[r] = __builtin_amdgcn_exp2f(l[r] - mx); sum += l[r]; }
      sum += __shfl_xor(sum, 16);
      sum += __shfl_xor(sum, 32);
      const float inv = __builtin_amdgcn_rcpf(sum);
      const bf8 pf = __builtin_bit_cast(bf8, (u32x4){pk2(l[0] * inv, l[1] * inv), pk2(l[2] * inv, l[3] * inv),
                                                     pk2(l[4] * inv, l[5] * inv), pk2(l[6] * inv, l[7] * inv)});
      const f32x4 o0 = __builtin_amdgcn_mfma_f32_16x16x32_bf16(v0, pf, z4, 0, 0, 0);
      const f32x4 o1 = __builtin_amdgcn_mfma_f32_16x16x32_bf16(v1, pf, z4, 0, 0, 0);
      const int gr = rbase + mt * 16 + m;
      const int k = cg * 80 + h * 20;
      if (gr < S_) {
        *(u32x2*)(A2 + a2_off(gr, k + 4 * g)) = (u32x2){pk2(o0[0], o0[1]), pk2(o0[2], o0[3])};
        if (g == 0) *(u32x2*)(A2 + a2_off(gr, k + 16)) = (u32x2){pk2(o1[0], o1[1]), pk2(o1[2], o1[3])};
      }
    }
  }
}

__global__ __launch_bounds__(512, 2) void phase1(const char* __restrict__ x, const char* __restrict__ wqkv,
                                                 const char* __restrict__ wo, const char* __restrict__ wg,
                                                 const char* __restrict__ wu, const char* __restrict__ wd,
                                                 char* __restrict__ A2, char* __restrict__ B2) {
  __shared__ __attribute__((aligned(1024))) char smem[LDS1_BYTES];
  const unsigned lds0 = (unsigned)(uintptr_t)(LDS_AS char*)smem;
  const int b = blockIdx.x, xcd = b & 7, slot = b >> 3;
  if (slot < 12) {
    const int hg = 2 * (xcd & 3) + slot / 6, rb = 6 * (xcd >> 2) + slot % 6;
    phase1_body<1>(x, wqkv, wqkv, wo, wd, A2, B2, rb, hg, lds0, (xcd * 12 + slot) * 3);
  } else {
    const int L = xcd * 20 + (slot - 12);
    if (L >= 153) return;
    int rb, cg;
    if (L < 72) { cg = L >> 3; rb = L & 7; } else { cg = (L - 72) / 9; rb = 8 + (L - 72) % 9; }
    phase1_body<0>(x, wg, wu, wo, wd, A2, B2, rb, cg, lds0, (96 + L) * 3);
  }
}

__global__ __launch_bounds__(256, 1) void phase2(const char* __restrict__ A2, const char* __restrict__ B2,
                                                 const char* __restrict__ x, char* __restrict__ out) {
  __shared__ __attribute__((aligned(16))) f32x4 red[4][4][5][64];
  const int tid = threadIdx.x, lane = tid & 63;
  const int w = __builtin_amdgcn_readfirstlane(tid >> 6);
  const int g = lane >> 4, m = lane & 15;
  const int b = blockIdx.x, xcd = b & 7, slot = b >> 3;
  const int rb = (xcd >> 1) * 8 + (slot & 7), cg = (xcd & 1) * 4 + (slot >> 3);
  const char* ap = A2 + (size_t)(rb * KS2 + w) * 4096 + lane * 16;
  const char* bp = B2 + (size_t)(cg * KS2 + w) * 5120 + lane * 16;
  constexpr int DEP = 4, NI = 19;
  u32x4 af[DEP][4], bfr[DEP][5];
  f32x4 acc[4][5];
#pragma unroll
  for (int i = 0; i < 4; ++i)
#pragma unroll
    for (int n = 0; n < 5; ++n) acc[i][n] = (f32x4){0.f, 0.f, 0.f, 0.f};
  const int last = w < 2 ? 18 : 17;
  auto load = [&](int i) {
    const int ii = i == NI - 1 ? last : i;
#pragma unroll
    for (int j = 0; j < 4; ++j) af[i % DEP][j] = *(const u32x4*)(ap + (size_t)ii * (4 * 4096) + j * 1024);
#pragma unroll
    for (int n = 0; n < 5; ++n) bfr[i % DEP][n] = *(const u32x4*)(bp + (size_t)ii * (4 * 5120) + n * 1024);
  };
  const int orow = rb * 64 + w * 16 + m;
  const size_t ooff = (size_t)orow * 1280 + (cg * 80 + 4 * g) * 2;
  u32x2 xr[5];
#pragma unroll
  for (int i = 0; i < DEP - 1; ++i) load(i);
#pragma unroll
  for (int n = 0; n < 5; ++n) xr[n] = *(const u32x2*)(x + ooff + n * 32);
#pragma unroll
  for (int i = 0; i < NI; ++i) {
    if (i + DEP - 1 < NI) load(i + DEP - 1);
    if (i < NI - 1 || w < 2) {
#pragma unroll
      for (int j = 0; j < 4; ++j)
#pragma unroll
        for (int n = 0; n < 5; ++n)
          acc[j][n] = __builtin_amdgcn_mfma_f32_16x16x32_bf16(__builtin_bit_cast(bf8, bfr[i % DEP][n]),
                                                              __builtin_bit_cast(bf8, af[i % DEP][j]), acc[j][n], 0, 0, 0);
    }
  }
#pragma unroll
  for (int j = 0; j < 4; ++j)
#pragma unroll
    for (int n = 0; n < 5; ++n) red[w][j][n][lane] = acc[j][n];
  __syncthreads();
#pragma unroll
  for (int n = 0; n < 5; ++n) {
    f32x4 s = red[0][w][n][lane];
    s += red[1][w][n][lane];
    s += red[2][w][n][lane];
    s += red[3][w][n][lane];
    const float x0 = __builtin_bit_cast(float, xr[n][0] << 16), x1 = __builtin_bit_cast(float, xr[n][0] & 0xffff0000u);
    const float x2 = __builtin_bit_cast(float, xr[n][1] << 16), x3 = __builtin_bit_cast(float, xr[n][1] & 0xffff0000u);
    *(u32x2*)(out + ooff + n * 32) = (u32x2){pk2(s[0] + x0, s[1] + x1), pk2(s[2] + x2, s[3] + x3)};
  }
}

extern "C" void kernel_launch(void* const* d_in, const int* in_sizes, int n_in, void* d_out, int out_size, void* d_ws,
                              size_t ws_size, hipStream_t stream) {
  const char* x = (const char*)d_in[0];
  const char* wqkv = (const char*)d_in[1];
  const char* wo = (const char*)d_in[2];
  const char* wg = (const char*)d_in[4];
  const char* wu = (const char*)d_in[5];
  const char* wd = (const char*)d_in[6];
  char* A2 = (char*)d_ws;
  char* B2 = A2 + A2_BYTES;
  phase1<<<256, 512, 0, stream>>>(x, wqkv, wo, wg, wu, wd, A2, B2);
  phase2<<<256, 256, 0, stream>>>(A2, B2, x, (char*)d_out);
}
```

```cpp
#include <hip/hip_runtime.h>
#include <hip/hip_bf16.h>
#include <stdint.h>

typedef __attribute__((ext_vector_type(4))) short s16x4;
typedef __attribute__((ext_vector_type(8))) short s16x8;
typedef __attribute__((ext_vector_type(2))) __bf16 bf2;
typedef __attribute__((ext_vector_type(8))) __bf16 bf8;
typedef __attribute__((ext_vector_type(4))) float f32x4;
typedef __attribute__((ext_vector_type(4))) unsigned u32x4;
typedef __attribute__((ext_vector_type(2))) unsigned u32x2;
#define LDS_AS __attribute__((address_space(3)))
#define DEVI __device__ __forceinline__

constexpr int S_ = 2048, D_ = 640, F_ = 1728;
constexpr int KS2 = 74;
constexpr size_t A2_BYTES = (size_t)32 * KS2 * 4096;
constexpr int STAT_OFF = 147456;
constexpr int LDS1_BYTES = STAT_OFF + 3328;
constexpr int QRS = 488;

#define STAMP(i)
#define STAMP2(i)
template <int ROLE> struct Cfg;
template <> struct Cfg<0> {
  static constexpr int WM = 2, WN = 4, MT = 4, NT = 6, WROW = 384, WSTAGE = 24576, XTILE = 16384,
                       XOFF = 98304, WI = 3, XE = 1, XO = 1;
};
template <> struct Cfg<1> {
  static constexpr int WM = 4, WN = 2, MT = 3, NT = 8, WROW = 512, WSTAGE = 16384, XTILE = 24576,
                       XOFF = 65536, WI = 2, XE = 2, XO = 1;
};
typedef __attribute__((ext_vector_type(2))) float f32x2;

DEVI void dma16(const char* sbase, unsigned voff, unsigned lds_off) {
  asm volatile("s_mov_b32 m0, %0\n\ts_nop 0\n\tglobal_load_lds_dwordx4 %1, %2" ::"s"(lds_off), "v"(voff), "s"(sbase) : "memory");
}
DEVI void dma16m(const char* sbase, unsigned voff, unsigned lds_off, unsigned long long mask) {
  asm volatile("s_mov_b32 m0, %0\n\ts_mov_b64 exec, %3\n\tglobal_load_lds_dwordx4 %1, %2\n\ts_mov_b64 exec, -1"
               ::"s"(lds_off), "v"(voff), "s"(sbase), "s"(mask) : "memory");
}
#define SB0 __builtin_amdgcn_sched_barrier(0)
template <int N> DEVI void wait_vm_lgkm0() {
  asm volatile("" ::: "memory");
  __builtin_amdgcn_s_waitcnt((N & 15) | ((N >> 4) << 14) | (7 << 4));
  asm volatile("" ::: "memory");
}
DEVI s16x4 tr4(unsigned a) { return __builtin_amdgcn_ds_read_tr16_b64_v4i16((LDS_AS s16x4*)(uintptr_t)a); }
DEVI bf8 cat8(s16x4 lo, s16x4 hi) { return __builtin_bit_cast(bf8, __builtin_shufflevector(lo, hi, 0, 1, 2, 3, 4, 5, 6, 7)); }
DEVI bf8 lds_b128(unsigned a) { return __builtin_bit_cast(bf8, *(LDS_AS u32x4*)(uintptr_t)a); }
DEVI u32x2 lds_b64(unsigned a) { return *(LDS_AS u32x2*)(uintptr_t)a; }
DEVI unsigned pk2(float a, float b) {
  bf2 v = {(__bf16)a, (__bf16)b};
  return __builtin_bit_cast(unsigned, v);
}
DEVI f32x4 mfma(bf8 a, bf8 b, f32x4 c) { return __builtin_amdgcn_mfma_f32_16x16x32_bf16(a, b, c, 0, 0, 0); }
DEVI float fbits(unsigned u) { return __builtin_bit_cast(float, u); }
DEVI unsigned ubits(float f) { return __builtin_bit_cast(unsigned, f); }
DEVI float red_max(float v) {
  auto a = __builtin_amdgcn_permlane16_swap(ubits(v), ubits(v), false, false);
  v = fmaxf(fbits(a[0]), fbits(a[1]));
  auto b = __builtin_amdgcn_permlane32_swap(ubits(v), ubits(v), false, false);
  return fmaxf(fbits(b[0]), fbits(b[1]));
}
DEVI float red_sum(float v) {
  auto a = __builtin_amdgcn_permlane16_swap(ubits(v), ubits(v), false, false);
  v = fbits(a[0]) + fbits(a[1]);
  auto b = __builtin_amdgcn_permlane32_swap(ubits(v), ubits(v), false, false);
  return fbits(b[0]) + fbits(b[1]);
}
DEVI unsigned a2_off(int r, int k) {
  return (unsigned)(((r >> 6) * KS2 + (k >> 5)) * 4096 + ((r >> 4) & 3) * 1024 + ((((k >> 3) & 3) * 16 + (r & 15)) * 16) +
                    ((k >> 2) & 1) * 8);
}
template <int ROLE> constexpr int n_issue(int t) {
  return (t <= 15 ? Cfg<ROLE>::WI : 0) + (t <= 14 ? (((t + 5) & 1) ? Cfg<ROLE>::XO : Cfg<ROLE>::XE) : 0);
}

template <int ROLE>
DEVI void phase1_body(const char* __restrict__ x, const char* __restrict__ wa, const char* __restrict__ wb,
                      const char* __restrict__ wo, const char* __restrict__ wd, char* __restrict__ A2,
                      char* __restrict__ B2, int rb, int cg, unsigned lds0, int job) {
  using C = Cfg<ROLE>;
  constexpr int MT = C::MT, NT = C::NT;
  const int tid = threadIdx.x, lane = tid & 63;
  const int w = __builtin_amdgcn_readfirstlane(tid >> 6);
  const int g = lane >> 4, m = lane & 15, q = m >> 2, p = lane & 3;
  const int wm = w / C::WN, wn = w % C::WN;
  const int rbase = ROLE == 0 ? rb * 127 - 1 : rb * 176 - 16;
  STAMP(0);

  unsigned wof[C::WI];
  unsigned long long wmask[C::WI];
  constexpr int WSTRIDE = ROLE == 0 ? F_ * 2 : 3 * D_ * 2;
  const char* wbase;
  if (ROLE == 0) {
    wbase = (w < 4 ? wa : wb) + cg * 384;
#pragma unroll
    for (int i = 0; i < C::WI; ++i) {
      const int idx = ((w & 3) * 3 + i) * 64 + lane;
      const int row = idx / 24, pc = idx % 24;
      const int f = ((row >> 1) & 1) | ((row >> 2) & 2);
      wof[i] = row * WSTRIDE + (((pc >> 1) ^ f) * 32) + (pc & 1) * 16;
    }
  } else {
    wbase = wa + cg * 160;
#pragma unroll
    for (int i = 0; i < C::WI; ++i) {
      const int row = 4 * w + 2 * i + (lane >> 5), pc = lane & 31;
      const int f = (row & 3) | ((row >> 1) & 4);
      const int c = 2 * ((pc >> 1) ^ f) + (pc & 1);
      const int part = c / 10, cc = c % 10;
      const int poff = part == 0 ? 1280 : (part == 1 ? 2560 : 0);
      wof[i] = row * WSTRIDE + poff + cc * 16;
      wmask[i] = __ballot(c < 30);
    }
  }
  constexpr int XI = C::XE + C::XO;
  unsigned xof[XI];
#pragma unroll
  for (int i = 0; i < XI; ++i) {
    const int lr = (i * 8 + w) * 8 + (lane >> 3);
    int gr = rbase + lr;
    gr = gr < 0 ? 0 : (gr > S_ - 1 ? S_ - 1 : gr);
    xof[i] = gr * 1280 + ((lane & 7) ^ ((lr >> 1) & 7)) * 16;
  }
  const unsigned long long xmask0 = w == 0 ? ~0xffull : ~0ull;
  const unsigned wdst = lds0 + w * (C::WI * 1024);
  const unsigned xdst = lds0 + C::XOFF + w * 1024;
  auto dma_w = [&](int d, int stage) {
    const char* b = wbase + (size_t)stage * (32 * WSTRIDE);
    if (ROLE == 0) dma16(b, wof[d], wdst + (stage & 3) * C::WSTAGE + d * 1024);
    else dma16m(b, wof[d], wdst + (stage & 3) * C::WSTAGE + d * 1024, wmask[d]);
  };
  auto dma_x = [&](int i, int tile, int buf) {
    if (i == 0) dma16m(x + tile * 128, xof[i], xdst + buf * C::XTILE, xmask0);
    else dma16(x + tile * 128, xof[i], xdst + buf * C::XTILE + i * 8192);
  };
  auto issue_w = [&](int stage) {
#pragma unroll
    for (int i = 0; i < C::WI; ++i) dma_w(i, stage);
  };
  auto issue_x = [&](int odd, int tile) {
#pragma unroll
    for (int i = (odd ? C::XE : 0); i < (odd ? XI : C::XE); ++i) dma_x(i, tile, tile);
  };
  issue_x(0, 0);
  issue_w(0); issue_x(1, 0);
  issue_w(1); issue_x(0, 1);
  issue_w(2); issue_x(1, 1);
  issue_w(3); issue_x(0, 2);
  const u32x4 ones4 = {0x3F803F80u, 0x3F803F80u, 0x3F803F80u, 0x3F803F80u};
  if (tid < 24) *(LDS_AS u32x4*)(uintptr_t)(lds0 + C::XOFF + (tid >> 3) * C::XTILE + (tid & 7) * 16) = ones4;
  if (ROLE == 1 && tid >= 256) {
    const int t2 = tid - 256, row = (t2 >> 1) & 31;
    const int f = (row & 3) | ((row >> 1) & 4);
    *(LDS_AS u32x4*)(uintptr_t)(lds0 + (t2 >> 6) * C::WSTAGE + row * 512 + (15 ^ f) * 32 + (t2 & 1) * 16) = ones4;
  }

  constexpr int NWA = ROLE == 0 ? 1 : 4;
  unsigned wad[NWA], xad[MT];
  if (ROLE == 0) {
    const int f = ((q >> 1) & 1) | ((g & 1) << 1);
    wad[0] = lds0 + (8 * g + q) * 384 + ((wn ^ f) * 32) + p * 8;
  } else {
    const int f = q | ((g & 1) << 2);
#pragma unroll
    for (int e = 0; e < NWA; ++e) wad[e] = lds0 + (8 * g + q) * 512 + (((wn * 4 + e) ^ f) * 32) + p * 8;
  }
  int lrow[MT];
#pragma unroll
  for (int j = 0; j < MT; ++j) {
    const int act = ROLE == 0 ? ((j + wn) & 3) : (wn ? 2 - j : j);
    lrow[j] = (wm * MT + act) * 16 + m;
    xad[j] = lds0 + C::XOFF + lrow[j] * 128 + ((g ^ ((lrow[j] >> 1) & 7)) * 16);
  }
  f32x4 acc[MT][NT];
#pragma unroll
  for (int j = 0; j < MT; ++j)
#pragma unroll
    for (int n = 0; n < NT; ++n) acc[j][n] = (f32x4){0.f, 0.f, 0.f, 0.f};
  f32x4 st0 = {0.f, 0.f, 0.f, 0.f}, st1 = {0.f, 0.f, 0.f, 0.f};
  const bf8 onesf = __builtin_bit_cast(bf8, ones4);
  bf8 xf[2][MT];
  s16x4 wh[2][NT][2];

  auto read_one = [&](int par, int r, unsigned woff, unsigned xoff) {
    if (r < MT) {
      xf[par][r] = lds_b128((xad[r] + xoff) ^ (par ? 64u : 0u));
    } else {
      const int n = (r - MT) >> 1, hl = (r - MT) & 1;
      if (ROLE == 0) wh[par][n][hl] = tr4(wad[0] + woff + (n & 1) * 12288 + (n >> 1) * 128 + hl * 4 * 384);
      else wh[par][n][hl] = tr4(wad[n & 3] + woff + (n >> 2) * 256 + hl * 4 * 512);
    }
  };
  constexpr int NR = MT + 2 * NT;

  wait_vm_lgkm0<n_issue<ROLE>(-3) + n_issue<ROLE>(-2) + n_issue<ROLE>(-1)>();
  __builtin_amdgcn_s_barrier();
#pragma unroll
  for (int r = 0; r < NR; ++r) read_one(0, r, 0, 0);
  STAMP(1);

  int xr = 0;
#define ITER(T, PAR, VM, DOW, DOX, DORD)                                                    \
  {                                                                                          \
    if (DORD) { wait_vm_lgkm0<VM>(); __builtin_amdgcn_s_barrier(); }                         \
    SB0;                                                                                     \
    const int xn = xr == 2 ? 0 : xr + 1;                                                     \
    const int xq = xn == 2 ? 0 : xn + 1;                                                     \
    const unsigned woff = (((T) + 1) & 3) * C::WSTAGE, xoff = (PAR ? xn : xr) * C::XTILE;    \
    _Pragma("unroll") for (int i = 0; i < 24; ++i) {                                         \
      const int n = i / MT, j = i % MT;                                                      \
      acc[j][n] = mfma(cat8(wh[PAR][n][0], wh[PAR][n][1]), xf[PAR][j], acc[j][n]);           \
      if (DORD && i < 20 && i * NR / 20 != (i + 1) * NR / 20) read_one(PAR ^ 1, i * NR / 20, woff, xoff); \
      if (DOW && i % 6 == 2) {                                                               \
        const int d = i / 6;                                                                 \
        if (d < C::WI) dma_w(d, (T) + 4);                                                    \
        else if (DOX) {                                                                      \
          const int xi = (PAR ? 0 : C::XE) + (d - C::WI);                                    \
          if (xi < (PAR ? C::XE : XI)) dma_x(xi, ((T) + 5) >> 1, PAR ? xr : xq);             \
        }                                                                                    \
      }                                                                                      \
      if (i == 21) st0 = mfma(xf[PAR][0], xf[PAR][0], st0);                                  \
      if (i == 22) {                                                                         \
        if (ROLE == 0) st1 = mfma(onesf, xf[PAR][0], st1);                                   \
        else if (wn == PAR) st1 = mfma(xf[PAR][1], xf[PAR][1], st1);                         \
      }                                                                                      \
      SB0;                                                                                   \
    }                                                                                        \
    if (PAR) xr = xn;                                                                        \
  }
  constexpr int VMF = n_issue<ROLE>(0) + n_issue<ROLE>(1);
  for (int t = 0; t < 14; t += 2) {
    ITER(t, 0, VMF, true, true, true)
    ITER(t + 1, 1, VMF, true, true, true)
  }
  ITER(14, 0, VMF, true, true, true)
  ITER(15, 1, VMF, true, false, true)
  ITER(16, 0, n_issue<ROLE>(14) + n_issue<ROLE>(15), false, false, true)
  ITER(17, 1, n_issue<ROLE>(15), false, false, true)
  ITER(18, 0, 0, false, false, true)
  u32x2 jl[8];
  const bool has_job = (w < 3) && (job + w < 740);
  int jks = 0, jcb = 0;
  if (has_job) {
    const int J = job + w;
    jks = J / 10; jcb = J % 10;
    const char* src = (jks < 20 ? wo + (size_t)(jks * 32) * 1280 : wd + (size_t)(jks * 32 - 640) * 1280) +
                      (size_t)(8 * g) * 1280 + jcb * 128 + m * 8;
#pragma unroll
    for (int j = 0; j < 8; ++j) jl[j] = *(const u32x2*)(src + j * 1280);
  }
  SB0;
  ITER(19, 1, 0, false, false, false)
#undef ITER
  STAMP(2);

  LDS_AS float* rsum = (LDS_AS float*)(uintptr_t)(lds0 + STAT_OFF);
  LDS_AS float* rsq = rsum + 192;
  LDS_AS float* csum = rsum + 448;
  {
    const bool diag = g == q;
    const float d0 = p == 0 ? st0[0] : (p == 1 ? st0[1] : (p == 2 ? st0[2] : st0[3]));
    if (ROLE == 0) {
      if (diag) { rsq[lrow[0]] = d0; rsum[lrow[0]] = st1[0]; }
    } else {
      const float d1 = p == 0 ? st1[0] : (p == 1 ? st1[1] : (p == 2 ? st1[2] : st1[3]));
      if (diag) {
        rsq[lrow[0]] = d0;
        rsq[wn ? 192 + wm * 16 + m : lrow[1]] = d1;
        if (wn) {
#pragma unroll
          for (int j = 0; j < MT; ++j) rsum[lrow[j]] = acc[j][7][0];
        }
      }
    }
  }
  if (wm == 0) {
#pragma unroll
    for (int j = 0; j < MT; ++j) {
      if (lrow[j] == 0) {
#pragma unroll
        for (int n = 0; n < NT; ++n) *(LDS_AS f32x4*)(csum + (wn * NT + n) * 16 + 4 * g) = acc[j][n];
      }
    }
  }
  __syncthreads();
  float ra[MT], rbv[MT];
#pragma unroll
  for (int j = 0; j < MT; ++j) {
    const float mean = rsum[lrow[j]] * (1.f / D_);
    float sq = rsq[lrow[j]];
    if (ROLE == 1 && j == 1) sq += rsq[192 + wm * 16 + m];
    const float var = fmaxf(sq * (1.f / D_) - mean * mean, 0.f);
    ra[j] = __builtin_amdgcn_rsqf(var + 1e-5f);
    rbv[j] = -mean * ra[j];
  }
  STAMP(3);
  if (ROLE == 0) {
    unsigned so[2];
    bool sv[2];
#pragma unroll
    for (int jp = 0; jp < 2; ++jp) {
      const int lr = (g & 1) ? lrow[2 * jp + 1] : lrow[2 * jp];
      const int gr = rbase + lr;
      sv[jp] = lr >= 1 && gr < S_;
      so[jp] = a2_off(gr, 640 + cg * 192 + wn * 16 + (g >> 1) * 8);
    }
#pragma unroll
    for (int a = 0; a < 3; ++a) {
      const f32x4 cg4 = *(LDS_AS f32x4*)(csum + (wn * NT + 2 * a) * 16 + 4 * g);
      const f32x4 cu4 = *(LDS_AS f32x4*)(csum + (wn * NT + 2 * a + 1) * 16 + 4 * g);
#pragma unroll
      for (int jp = 0; jp < 2; ++jp) {
        unsigned hp[2][2];
#pragma unroll
        for (int jj = 0; jj < 2; ++jj) {
          const int j = 2 * jp + jj;
#pragma unroll
          for (int r = 0; r < 2; ++r) {
            const f32x2 ag = {acc[j][2 * a][2 * r], acc[j][2 * a][2 * r + 1]};
            const f32x2 au = {acc[j][2 * a + 1][2 * r], acc[j][2 * a + 1][2 * r + 1]};
            const f32x2 cgv = {cg4[2 * r], cg4[2 * r + 1]}, cuv = {cu4[2 * r], cu4[2 * r + 1]};
            const f32x2 gv = ag * ra[j] + cgv * rbv[j];
            const f32x2 uv = au * ra[j] + cuv * rbv[j];
            const f32x2 z = gv * (gv * gv * -0.10294324f - 2.3022082f);
            const f32x2 gu = gv * uv;
            const float h0 = gu[0] * __builtin_amdgcn_rcpf(1.f + __builtin_amdgcn_exp2f(z[0]));
            const float h1 = gu[1] * __builtin_amdgcn_rcpf(1.f + __builtin_amdgcn_exp2f(z[1]));
            hp[jj][r] = pk2(h0, h1);
          }
        }
        auto s0 = __builtin_amdgcn_permlane16_swap(hp[0][0], hp[1][0], false, false);
        auto s1 = __builtin_amdgcn_permlane16_swap(hp[0][1], hp[1][1], false, false);
        if (sv[jp]) *(u32x4*)(A2 + (so[jp] + a * 8192)) = (u32x4){s0[0], s1[0], s0[1], s1[1]};
      }
    }
  } else {
#pragma unroll
    for (int n = 0; n < NT; ++n) {
      const int tau = (n >> 2) * 8 + wn * 4 + (n & 3);
      const f32x4 c4 = *(LDS_AS f32x4*)(csum + (wn * NT + n) * 16 + 4 * g);
      if (tau < 15) {
#pragma unroll
        for (int j = 0; j < MT; ++j) {
          const f32x4 v = acc[j][n] * ra[j] + c4 * rbv[j];
          *(LDS_AS u32x2*)(uintptr_t)(lds0 + lrow[j] * QRS + tau * 32 + g * 8) = (u32x2){pk2(v[0], v[1]), pk2(v[2], v[3])};
        }
      }
    }
    __syncthreads();
    STAMP(4);
    const float sc = 0.22360679775f * 1.44269504089f;
    bool ok0[4], ok1[4];
#pragma unroll
    for (int r = 0; r < 4; ++r) {
      const int i = 4 * g + r;
      const bool par = ((m - i) & 1) == 0;
      ok0[r] = par && (i >= m + 2);
      ok1[r] = par && (i <= m);
    }
#pragma unroll
    for (int ii = 0; ii < 6; ++ii) {
      const int it0 = w + 8 * ii;
      const int it = it0 < 44 ? it0 : 43;
      const int mt = 1 + (it >> 2), h = it & 3;
      const unsigned rq = lds0 + (mt * 16 + m) * QRS + h * 40 + g * 16;
      const u32x2 k0a = lds_b64(rq - 16 * QRS), k0b = lds_b64(rq - 16 * QRS + 8);
      const u32x2 k1a = lds_b64(rq), k1b = lds_b64(rq + 8);
      u32x2 qa = lds_b64(rq + 320), qb = lds_b64(rq + 328);
      const unsigned va = lds0 + ((mt - 1) * 16 + 4 * g + q) * QRS + 160 + h * 40 + p * 8;
      const bf8 v0 = cat8(tr4(va), tr4(va + 16 * QRS));
      const bf8 v1 = cat8(tr4(va + 32), tr4(va + 16 * QRS + 32));
      if (g == 3) qa = (u32x2){0u, 0u};
      if (g >= 2) qb = (u32x2){0u, 0u};
      const bf8 qf = __builtin_bit_cast(bf8, (u32x4){qa[0], qa[1], qb[0], qb[1]});
      const f32x4 z4 = {0.f, 0.f, 0.f, 0.f};
      const f32x4 s0 = mfma(__builtin_bit_cast(bf8, (u32x4){k0a[0], k0a[1], k0b[0], k0b[1]}), qf, z4);
      const f32x4 s1 = mfma(__builtin_bit_cast(bf8, (u32x4){k1a[0], k1a[1], k1b[0], k1b[1]}), qf, z4);
      const bool prev_ok = (rbase + (mt - 1) * 16) >= 0;
      float l[8];
      float mx = -3.0e38f;
#pragma unroll
      for (int r = 0; r < 4; ++r) {
        l[r] = (ok0[r] && prev_ok) ? s0[r] * sc : -3.0e38f;
        l[4 + r] = ok1[r] ? s1[r] * sc : -3.0e38f;
        mx = fmaxf(mx, fmaxf(l[r], l[4 + r]));
      }
      mx = red_max(mx);
      float sum = 0.f;
#pragma unroll
      for (int r = 0; r < 8; ++r) { l[r] = __builtin_amdgcn_exp2f(l[r] - mx); sum += l[r]; }
      const float inv = __builtin_amdgcn_rcpf(red_sum(sum));
      const bf8 pf = __builtin_bit_cast(bf8, (u32x4){pk2(l[0] * inv, l[1] * inv), pk2(l[2] * inv, l[3] * inv),
                                                     pk2(l[4] * inv, l[5] * inv), pk2(l[6] * inv, l[7] * inv)});
      const f32x4 o0 = mfma(v0, pf, z4);
      const f32x4 o1 = mfma(v1, pf, z4);
      const int gr = rbase + mt * 16 + m;
      const int k = cg * 80 + h * 20;
      if (gr < S_ && it0 < 44) {
        *(u32x2*)(A2 + a2_off(gr, k + 4 * g)) = (u32x2){pk2(o0[0], o0[1]), pk2(o0[2], o0[3])};
        if (g == 0) *(u32x2*)(A2 + a2_off(gr, k + 16)) = (u32x2){pk2(o1[0], o1[1]), pk2(o1[2], o1[3])};
      }
    }
  }
  if (has_job) {
    const int n0 = jcb * 64 + 4 * m, nt = n0 >> 4;
    char* dst = B2 + (size_t)(((nt / 5) * KS2 + jks) * 5 + nt % 5) * 1024 + (g * 16 + (n0 & 15)) * 16;
#pragma unroll
    for (int i = 0; i < 4; ++i) {
      u32x4 o;
#pragma unroll
      for (int j = 0; j < 4; ++j)
        o[j] = __builtin_amdgcn_perm(jl[2 * j + 1][i >> 1], jl[2 * j][i >> 1], (i & 1) ? 0x07060302u : 0x05040100u);
      *(u32x4*)(dst + i * 16) = o;
    }
  }
  STAMP(5);
}

__global__ __launch_bounds__(512, 2) void phase1(const char* __restrict__ x, const char* __restrict__ wqkv,
                                                 const char* __restrict__ wo, const char* __restrict__ wg,
                                                 const char* __restrict__ wu, const char* __restrict__ wd,
                                                 char* __restrict__ A2, char* __restrict__ B2) {
  __shared__ __attribute__((aligned(1024))) char smem[LDS1_BYTES];
  const unsigned lds0 = (unsigned)(uintptr_t)(LDS_AS char*)smem;
  const int b = blockIdx.x, xcd = b & 7, slot = b >> 3;
  if (slot < 12) {
    const int hg = 2 * (xcd & 3) + slot / 6, rb = 6 * (xcd >> 2) + slot % 6;
    phase1_body<1>(x, wqkv, wqkv, wo, wd, A2, B2, rb, hg, lds0, (xcd * 12 + slot) * 3);
  } else {
    const int L = xcd * 20 + (slot - 12);
    if (L >= 153) return;
    int rb, cg;
    if (L < 72) { cg = L >> 3; rb = L & 7; } else { cg = (L - 72) / 9; rb = 8 + (L - 72) % 9; }
    phase1_body<0>(x, wg, wu, wo, wd, A2, B2, rb, cg, lds0, (96 + L) * 3);
  }
}

__global__ __launch_bounds__(256, 1) void phase2(const char* __restrict__ A2, const char* __restrict__ B2,
                                                 const char* __restrict__ x, char* __restrict__ out) {
  __shared__ __attribute__((aligned(16))) f32x4 red[4][4][5][64];
  const int tid = threadIdx.x, lane = tid & 63;
  const int w = __builtin_amdgcn_readfirstlane(tid >> 6);
  const int g = lane >> 4, m = lane & 15;
  const int b = blockIdx.x, xcd = b & 7, slot = b >> 3;
  const int rb = (xcd >> 1) * 8 + (slot & 7), cg = (xcd & 1) * 4 + (slot >> 3);
  const char* ap = A2 + (size_t)(rb * KS2 + w) * 4096 + lane * 16;
  const char* bp = B2 + (size_t)(cg * KS2 + w) * 5120 + lane * 16;
  STAMP2(0);
  constexpr int DEP = 4, NI = 19;
  u32x4 af[DEP][4], bfr[DEP][5];
  f32x4 acc[4][5];
#pragma unroll
  for (int i = 0; i < 4; ++i)
#pragma unroll
    for (int n = 0; n < 5; ++n) acc[i][n] = (f32x4){0.f, 0.f, 0.f, 0.f};
  const int last = w < 2 ? 18 : 17;
  auto load = [&](int i) {
    const int ii = i == NI - 1 ? last : i;
#pragma unroll
    for (int j = 0; j < 4; ++j) af[i % DEP][j] = *(const u32x4*)(ap + (size_t)ii * (4 * 4096) + j * 1024);
#pragma unroll
    for (int n = 0; n < 5; ++n) bfr[i % DEP][n] = *(const u32x4*)(bp + (size_t)ii * (4 * 5120) + n * 1024);
  };
  const int orow = rb * 64 + w * 16 + m;
  const size_t ooff = (size_t)orow * 1280 + (cg * 80 + 4 * g) * 2;
  u32x2 xr[5];
#pragma unroll
  for (int i = 0; i < DEP - 1; ++i) load(i);
#pragma unroll
  for (int n = 0; n < 5; ++n) xr[n] = *(const u32x2*)(x + ooff + n * 32);
  SB0;
#pragma unroll
  for (int i = 0; i < NI; ++i) {
    if (i + DEP - 1 < NI) load(i + DEP - 1);
    SB0;
    if (i < NI - 1 || w < 2) {
#pragma unroll
      for (int j = 0; j < 4; ++j)
#pragma unroll
        for (int n = 0; n < 5; ++n)
          acc[j][n] = __builtin_amdgcn_mfma_f32_16x16x32_bf16(__builtin_bit_cast(bf8, bfr[i % DEP][n]),
                                                              __builtin_bit_cast(bf8, af[i % DEP][j]), acc[j][n], 0, 0, 0);
    }
    SB0;
  }
  STAMP2(1);
#pragma unroll
  for (int j = 0; j < 4; ++j)
#pragma unroll
    for (int n = 0; n < 5; ++n) red[w][j][n][lane] = acc[j][n];
  __syncthreads();
#pragma unroll
  for (int n = 0; n < 5; ++n) {
    f32x4 s = red[0][w][n][lane];
    s += red[1][w][n][lane];
    s += red[2][w][n][lane];
    s += red[3][w][n][lane];
    const float x0 = __builtin_bit_cast(float, xr[n][0] << 16), x1 = __builtin_bit_cast(float, xr[n][0] & 0xffff0000u);
    const float x2 = __builtin_bit_cast(float, xr[n][1] << 16), x3 = __builtin_bit_cast(float, xr[n][1] & 0xffff0000u);
    *(u32x2*)(out + ooff + n * 32) = (u32x2){pk2(s[0] + x0, s[1] + x1), pk2(s[2] + x2, s[3] + x3)};
  }
  STAMP2(2);
}

extern "C" void kernel_launch(void* const* d_in, const int* in_sizes, int n_in, void* d_out, int out_size, void* d_ws,
                              size_t ws_size, hipStream_t stream) {
  const char* x = (const char*)d_in[0];
  const char* wqkv = (const char*)d_in[1];
  const char* wo = (const char*)d_in[2];
  const char* wg = (const char*)d_in[4];
  const char* wu = (const char*)d_in[5];
  const char* wd = (const char*)d_in[6];
  char* A2 = (char*)d_ws;
  char* B2 = A2 + A2_BYTES;
  phase1<<<256, 512, 0, stream>>>(x, wqkv, wo, wg, wu, wd, A2, B2);
  phase2<<<256, 256, 0, stream>>>(A2, B2, x, (char*)d_out);
}
```

```cpp
#include <hip/hip_runtime.h>
#include <hip/hip_bf16.h>
#include <stdint.h>

typedef __attribute__((ext_vector_type(4))) short s16x4;
typedef __attribute__((ext_vector_type(8))) short s16x8;
typedef __attribute__((ext_vector_type(2))) __bf16 bf2;
typedef __attribute__((ext_vector_type(8))) __bf16 bf8;
typedef __attribute__((ext_vector_type(4))) float f32x4;
typedef __attribute__((ext_vector_type(4))) unsigned u32x4;
typedef __attribute__((ext_vector_type(2))) unsigned u32x2;
#define LDS_AS __attribute__((address_space(3)))
#define DEVI __device__ __forceinline__

constexpr int S_ = 2048, D_ = 640, F_ = 1728;
constexpr int KS2 = 74;
constexpr size_t A2_BYTES = (size_t)32 * KS2 * 4096;
constexpr int STAT_OFF = 147456;
constexpr int LDS1_BYTES = STAT_OFF + 3328;
constexpr int QRS = 488;

#define STAMP(i)
#define STAMP2(i)
template <int ROLE> struct Cfg;
template <> struct Cfg<0> {
  static constexpr int WM = 2, WN = 4, MT = 4, NT = 6, WROW = 384, WSTAGE = 24576, XTILE = 16384,
                       XOFF = 98304, WI = 3, XE = 1, XO = 1;
};
template <> struct Cfg<1> {
  static constexpr int WM = 4, WN = 2, MT = 3, NT = 8, WROW = 512, WSTAGE = 16384, XTILE = 24576,
                       XOFF = 65536, WI = 2, XE = 2, XO = 1;
};
typedef __attribute__((ext_vector_type(2))) float f32x2;

DEVI void dma16(const char* sbase, unsigned voff, unsigned lds_off) {
  asm volatile("s_mov_b32 m0, %0\n\ts_nop 0\n\tglobal_load_lds_dwordx4 %1, %2" ::"s"(lds_off), "v"(voff), "s"(sbase) : "memory");
}
DEVI void dma16m(const char* sbase, unsigned voff, unsigned lds_off, unsigned long long mask) {
  asm volatile("s_mov_b32 m0, %0\n\ts_mov_b64 exec, %3\n\tglobal_load_lds_dwordx4 %1, %2\n\ts_mov_b64 exec, -1"
               ::"s"(lds_off), "v"(voff), "s"(sbase), "s"(mask) : "memory");
}
#define SB0 __builtin_amdgcn_sched_barrier(0)
template <int N> DEVI void wait_vm_lgkm0() {
  asm volatile("" ::: "memory");
  __builtin_amdgcn_s_waitcnt((N & 15) | ((N >> 4) << 14) | (7 << 4));
  asm volatile("" ::: "memory");
}
DEVI void lds_barrier() {
  asm volatile("" ::: "memory");
  __builtin_amdgcn_s_waitcnt(0xC07F);
  __builtin_amdgcn_s_barrier();
  asm volatile("" ::: "memory");
}
DEVI s16x4 tr4(unsigned a) { return __builtin_amdgcn_ds_read_tr16_b64_v4i16((LDS_AS s16x4*)(uintptr_t)a); }
DEVI bf8 cat8(s16x4 lo, s16x4 hi) { return __builtin_bit_cast(bf8, __builtin_shufflevector(lo, hi, 0, 1, 2, 3, 4, 5, 6, 7)); }
DEVI bf8 lds_b128(unsigned a) { return __builtin_bit_cast(bf8, *(LDS_AS u32x4*)(uintptr_t)a); }
DEVI u32x2 lds_b64(unsigned a) { return *(LDS_AS u32x2*)(uintptr_t)a; }
DEVI unsigned pk2(float a, float b) {
  bf2 v = {(__bf16)a, (__bf16)b};
  return __builtin_bit_cast(unsigned, v);
}
DEVI f32x4 mfma(bf8 a, bf8 b, f32x4 c) { return __builtin_amdgcn_mfma_f32_16x16x32_bf16(a, b, c, 0, 0, 0); }
DEVI float fbits(unsigned u) { return __builtin_bit_cast(float, u); }
DEVI unsigned ubits(float f) { return __builtin_bit_cast(unsigned, f); }
#define P2DEP 4
#define ST_AUX 0
typedef __amdgpu_buffer_rsrc_t rsrc_t;
DEVI rsrc_t mk_rsrc(void* p, unsigned bytes) { return __builtin_amdgcn_make_buffer_rsrc(p, 0, bytes, 0x00020000); }
DEVI void bst64(rsrc_t r, unsigned off, u32x2 v) { __builtin_amdgcn_raw_buffer_store_b64(v, r, off, 0, ST_AUX); }
DEVI void bst128(rsrc_t r, unsigned off, u32x4 v) { __builtin_amdgcn_raw_buffer_store_b128(v, r, off, 0, ST_AUX); }
constexpr unsigned OOB = 0x80000000u;
DEVI float red_max(float v) {
  auto a = __builtin_amdgcn_permlane16_swap(ubits(v), ubits(v), false, false);
  v = fmaxf(fbits(a[0]), fbits(a[1]));
  auto b = __builtin_amdgcn_permlane32_swap(ubits(v), ubits(v), false, false);
  return fmaxf(fbits(b[0]), fbits(b[1]));
}
DEVI float red_sum(float v) {
  auto a = __builtin_amdgcn_permlane16_swap(ubits(v), ubits(v), false, false);
  v = fbits(a[0]) + fbits(a[1]);
  auto b = __builtin_amdgcn_permlane32_swap(ubits(v), ubits(v), false, false);
  return fbits(b[0]) + fbits(b[1]);
}
DEVI unsigned a2_off(int r, int k) {
  return (unsigned)(((r >> 6) * KS2 + (k >> 5)) * 4096 + ((r >> 4) & 3) * 1024 + ((((k >> 3) & 3) * 16 + (r & 15)) * 16) +
                    ((k >> 2) & 1) * 8);
}
template <int ROLE> constexpr int n_issue(int t) {
  return (t <= 15 ? Cfg<ROLE>::WI : 0) + (t <= 14 ? (((t + 5) & 1) ? Cfg<ROLE>::XO : Cfg<ROLE>::XE) : 0);
}

template <int ROLE>
DEVI void phase1_body(const char* __restrict__ x, const char* __restrict__ wa, const char* __restrict__ wb,
                      const char* __restrict__ wo, const char* __restrict__ wd, char* __restrict__ A2,
                      char* __restrict__ B2, int rb, int cg, unsigned lds0, int job) {
  using C = Cfg<ROLE>;
  constexpr int MT = C::MT, NT = C::NT;
  const int tid = threadIdx.x, lane = tid & 63;
  const int w = __builtin_amdgcn_readfirstlane(tid >> 6);
  const int g = lane >> 4, m = lane & 15, q = m >> 2, p = lane & 3;
  const int wm = w / C::WN, wn = w % C::WN;
  const int rbase = ROLE == 0 ? rb * 127 - 1 : rb * 176 - 16;
  STAMP(0);

  unsigned wof[C::WI];
  unsigned long long wmask[C::WI];
  constexpr int WSTRIDE = ROLE == 0 ? F_ * 2 : 3 * D_ * 2;
  const char* wbase;
  if (ROLE == 0) {
    wbase = (w < 4 ? wa : wb) + cg * 384;
#pragma unroll
    for (int i = 0; i < C::WI; ++i) {
      const int idx = ((w & 3) * 3 + i) * 64 + lane;
      const int row = idx / 24, pc = idx % 24;
      const int f = ((row >> 1) & 1) | ((row >> 2) & 2);
      wof[i] = row * WSTRIDE + (((pc >> 1) ^ f) * 32) + (pc & 1) * 16;
    }
  } else {
    wbase = wa + cg * 160;
#pragma unroll
    for (int i = 0; i < C::WI; ++i) {
      const int row = 4 * w + 2 * i + (lane >> 5), pc = lane & 31;
      const int f = (row & 3) | ((row >> 1) & 4);
      const int c = 2 * ((pc >> 1) ^ f) + (pc & 1);
      const int part = c / 10, cc = c % 10;
      const int poff = part == 0 ? 1280 : (part == 1 ? 2560 : 0);
      wof[i] = row * WSTRIDE + poff + cc * 16;
      wmask[i] = __ballot(c < 30);
    }
  }
  constexpr int XI = C::XE + C::XO;
  unsigned xof[XI];
#pragma unroll
  for (int i = 0; i < XI; ++i) {
    const int lr = (i * 8 + w) * 8 + (lane >> 3);
    int gr = rbase + lr;
    gr = gr < 0 ? 0 : (gr > S_ - 1 ? S_ - 1 : gr);
    xof[i] = gr * 1280 + ((lane & 7) ^ ((lr >> 1) & 7)) * 16;
  }
  const unsigned long long xmask0 = w == 0 ? ~0xffull : ~0ull;
  const unsigned wdst = lds0 + w * (C::WI * 1024);
  const unsigned xdst = lds0 + C::XOFF + w * 1024;
  auto dma_w = [&](int d, int stage) {
    const char* b = wbase + (size_t)stage * (32 * WSTRIDE);
    if (ROLE == 0) dma16(b, wof[d], wdst + (stage & 3) * C::WSTAGE + d * 1024);
    else dma16m(b, wof[d], wdst + (stage & 3) * C::WSTAGE + d * 1024, wmask[d]);
  };
  auto dma_x = [&](int i, int tile, int buf) {
    if (i == 0) dma16m(x + tile * 128, xof[i], xdst + buf * C::XTILE, xmask0);
    else dma16(x + tile * 128, xof[i], xdst + buf * C::XTILE + i * 8192);
  };
  auto issue_w = [&](int stage) {
#pragma unroll
    for (int i = 0; i < C::WI; ++i) dma_w(i, stage);
  };
  auto issue_x = [&](int odd, int tile) {
#pragma unroll
    for (int i = (odd ? C::XE : 0); i < (odd ? XI : C::XE); ++i) dma_x(i, tile, tile);
  };
  issue_x(0, 0);
  issue_w(0); issue_x(1, 0);
  issue_w(1); issue_x(0, 1);
  const u32x4 ones4 = {0x3F803F80u, 0x3F803F80u, 0x3F803F80u, 0x3F803F80u};
  if (tid < 24) *(LDS_AS u32x4*)(uintptr_t)(lds0 + C::XOFF + (tid >> 3) * C::XTILE + (tid & 7) * 16) = ones4;
  if (ROLE == 1 && tid >= 256) {
    const int t2 = tid - 256, row = (t2 >> 1) & 31;
    const int f = (row & 3) | ((row >> 1) & 4);
    *(LDS_AS u32x4*)(uintptr_t)(lds0 + (t2 >> 6) * C::WSTAGE + row * 512 + (15 ^ f) * 32 + (t2 & 1) * 16) = ones4;
  }

  constexpr int NWA = ROLE == 0 ? 1 : 4;
  unsigned wad[NWA], xad[MT];
  if (ROLE == 0) {
    const int f = ((q >> 1) & 1) | ((g & 1) << 1);
    wad[0] = lds0 + (8 * g + q) * 384 + ((wn ^ f) * 32) + p * 8;
  } else {
    const int f = q | ((g & 1) << 2);
#pragma unroll
    for (int e = 0; e < NWA; ++e) wad[e] = lds0 + (8 * g + q) * 512 + (((wn * 4 + e) ^ f) * 32) + p * 8;
  }
  int lrow[MT];
#pragma unroll
  for (int j = 0; j < MT; ++j) {
    const int act = ROLE == 0 ? ((j + wn) & 3) : (wn ? 2 - j : j);
    lrow[j] = (wm * MT + act) * 16 + m;
    xad[j] = lds0 + C::XOFF + lrow[j] * 128 + ((g ^ ((lrow[j] >> 1) & 7)) * 16);
  }
  f32x4 acc[MT][NT];
#pragma unroll
  for (int j = 0; j < MT; ++j)
#pragma unroll
    for (int n = 0; n < NT; ++n) acc[j][n] = (f32x4){0.f, 0.f, 0.f, 0.f};
  f32x4 st0 = {0.f, 0.f, 0.f, 0.f}, st1 = {0.f, 0.f, 0.f, 0.f};
  const bf8 onesf = __builtin_bit_cast(bf8, ones4);
  bf8 xf[2][MT];
  s16x4 wh[2][NT][2];

  auto read_one = [&](int par, int r, unsigned woff, unsigned xoff) {
    if (r < MT) {
      xf[par][r] = lds_b128((xad[r] + xoff) ^ (par ? 64u : 0u));
    } else {
      const int n = (r - MT) >> 1, hl = (r - MT) & 1;
      if (ROLE == 0) wh[par][n][hl] = tr4(wad[0] + woff + (n & 1) * 12288 + (n >> 1) * 128 + hl * 4 * 384);
      else wh[par][n][hl] = tr4(wad[n & 3] + woff + (n >> 2) * 256 + hl * 4 * 512);
    }
  };
  constexpr int NR = MT + 2 * NT;

  wait_vm_lgkm0<n_issue<ROLE>(-3)>();
  __builtin_amdgcn_s_barrier();
#pragma unroll
  for (int r = 0; r < NR; ++r) read_one(0, r, 0, 0);
  STAMP(1);

  int xr = 0;
#define JOB_HOOK
#define ITER(T, PAR, VM, DOW, DOX, DORD, DBL)                                                    \
  {                                                                                          \
    if (DORD) { wait_vm_lgkm0<VM>(); __builtin_amdgcn_s_barrier(); }                         \
    SB0;                                                                                     \
    const int xn = xr == 2 ? 0 : xr + 1;                                                     \
    const int xq = xn == 2 ? 0 : xn + 1;                                                     \
    const unsigned woff = (((T) + 1) & 3) * C::WSTAGE, xoff = (PAR ? xn : xr) * C::XTILE;    \
    _Pragma("unroll") for (int i = 0; i < 24; ++i) {                                         \
      const int n = i / MT, j = i % MT;                                                      \
      acc[j][n] = mfma(cat8(wh[PAR][n][0], wh[PAR][n][1]), xf[PAR][j], acc[j][n]);           \
      if (DORD && i < 20 && i * NR / 20 != (i + 1) * NR / 20) read_one(PAR ^ 1, i * NR / 20, woff, xoff); \
      if (DBL && i % 3 == 2) {     \
        const int V = 2 * (PAR) - 2 + (i / 12), d = (i / 3) & 3, pc_ = V + 5;                \
        if (d < C::WI) dma_w(d, V + 4);                                                      \
        else {                                                                               \
          const int xi = ((pc_ & 1) ? C::XE : 0) + (d - C::WI);                              \
          if (xi < ((pc_ & 1) ? XI : C::XE)) dma_x(xi, pc_ >> 1, (pc_ >> 1) % 3);            \
        }                                                                                    \
      }                                                                                      \
      if (!DBL && DOW && i % 6 == 2) {                                                       \
        const int d = i / 6;                                                                 \
        if (d < C::WI) dma_w(d, (T) + 4);                                                    \
        else if (DOX) {                                                                      \
          const int xi = (PAR ? 0 : C::XE) + (d - C::WI);                                    \
          if (xi < (PAR ? C::XE : XI)) dma_x(xi, ((T) + 5) >> 1, PAR ? xr : xq);             \
        }                                                                                    \
      }                                                                                      \
      JOB_HOOK                                                                               \
      if (i == 21) st0 = mfma(xf[PAR][0], xf[PAR][0], st0);                                  \
      if (i == 22) {                                                                         \
        if (ROLE == 0) st1 = mfma(onesf, xf[PAR][0], st1);                                   \
        else if (wn == PAR) st1 = mfma(xf[PAR][1], xf[PAR][1], st1);                         \
      }                                                                                      \
      SB0;                                                                                   \
    }                                                                                        \
    if (PAR) xr = xn;                                                                        \
  }
  constexpr int VMF = n_issue<ROLE>(0) + n_issue<ROLE>(1);
  ITER(0, 0, 0, true, true, true, true)
  ITER(1, 1, n_issue<ROLE>(-1), true, true, true, true)
  for (int t = 2; t < 14; t += 2) {
    ITER(t, 0, VMF, true, true, true, false)
    ITER(t + 1, 1, VMF, true, true, true, false)
  }
  ITER(14, 0, VMF, true, true, true, false)
  ITER(15, 1, VMF, true, false, true, false)
  ITER(16, 0, n_issue<ROLE>(14) + n_issue<ROLE>(15), false, false, true, false)
  ITER(17, 1, n_issue<ROLE>(15), false, false, true, false)
  unsigned jl[8];
  const int J = job + w < 1480 ? job + w : 1479;
  const int jks = J / 20, jcb = J % 20;
  const char* jsrc = (jks < 20 ? wo + (size_t)(jks * 32) * 1280 : wd + (size_t)(jks * 32 - 640) * 1280) + jcb * 64;
#undef JOB_HOOK
#define JOB_HOOK if (i < 8) jl[i] = *(const unsigned*)(jsrc + (unsigned)((8 * g + i) * 1280 + m * 4));
  ITER(18, 0, 0, false, false, true, false)
#undef JOB_HOOK
#define JOB_HOOK
  ITER(19, 1, 0, false, false, false, false)
#undef ITER
#undef JOB_HOOK
  STAMP(2);

  LDS_AS float* rsum = (LDS_AS float*)(uintptr_t)(lds0 + STAT_OFF);
  LDS_AS float* rsq = rsum + 192;
  LDS_AS float* csum = rsum + 448;
  {
    const bool diag = g == q;
    const float d0 = p == 0 ? st0[0] : (p == 1 ? st0[1] : (p == 2 ? st0[2] : st0[3]));
    if (ROLE == 0) {
      if (diag) { rsq[lrow[0]] = d0; rsum[lrow[0]] = st1[0]; }
    } else {
      const float d1 = p == 0 ? st1[0] : (p == 1 ? st1[1] : (p == 2 ? st1[2] : st1[3]));
      if (diag) {
        rsq[lrow[0]] = d0;
        rsq[wn ? 192 + wm * 16 + m : lrow[1]] = d1;
        if (wn) {
#pragma unroll
          for (int j = 0; j < MT; ++j) rsum[lrow[j]] = acc[j][7][0];
        }
      }
    }
  }
  if (wm == 0) {
#pragma unroll
    for (int j = 0; j < MT; ++j) {
      if (lrow[j] == 0) {
#pragma unroll
        for (int n = 0; n < NT; ++n) *(LDS_AS f32x4*)(csum + (wn * NT + n) * 16 + 4 * g) = acc[j][n];
      }
    }
  }
  lds_barrier();
  float ra[MT], rbv[MT], ir2[MT], k1[MT], k3[MT];
#pragma unroll
  for (int j = 0; j < MT; ++j) {
    const float mean = rsum[lrow[j]] * (1.f / D_);
    float sq = rsq[lrow[j]];
    if (ROLE == 1 && j == 1) sq += rsq[192 + wm * 16 + m];
    const float var = fmaxf(sq * (1.f / D_) - mean * mean, 0.f);
    ra[j] = __builtin_amdgcn_rsqf(var + 1e-5f);
    rbv[j] = ROLE == 0 ? -mean : -mean * ra[j];
    ir2[j] = var + 1e-5f;
    k1[j] = -2.3022082f * ra[j];
    k3[j] = -0.10294324f * ra[j] * ra[j] * ra[j];
  }
  STAMP(3);
  {
    const int n0 = jcb * 32 + 2 * m, nt = n0 >> 4;
    const rsrc_t b2r = mk_rsrc(B2, 8 * KS2 * 5120);
    const unsigned dst = (unsigned)((((nt / 5) * KS2 + jks) * 5 + nt % 5) * 1024 + (g * 16 + (n0 & 15)) * 16);
#pragma unroll
    for (int i = 0; i < 2; ++i) {
      u32x4 o;
#pragma unroll
      for (int j = 0; j < 4; ++j) o[j] = __builtin_amdgcn_perm(jl[2 * j + 1], jl[2 * j], i ? 0x07060302u : 0x05040100u);
      bst128(b2r, dst + i * 16, o);
    }
  }
  const rsrc_t a2r = mk_rsrc(A2, (unsigned)A2_BYTES);
  if (ROLE == 0) {
    unsigned so[2];
#pragma unroll
    for (int jp = 0; jp < 2; ++jp) {
      const int lr = (wm * 4 + ((2 * jp + (g & 1) + wn) & 3)) * 16 + m;
      const int gr = rbase + lr;
      so[jp] = (lr >= 1 && gr < S_) ? a2_off(gr, 640 + cg * 192 + wn * 16 + (g >> 1) * 8) : OOB;
    }
#pragma unroll
    for (int a = 0; a < 3; ++a) {
      const f32x4 cg4 = *(LDS_AS f32x4*)(csum + (wn * NT + 2 * a) * 16 + 4 * g);
      const f32x4 cu4 = *(LDS_AS f32x4*)(csum + (wn * NT + 2 * a + 1) * 16 + 4 * g);
#pragma unroll
      for (int jp = 0; jp < 2; ++jp) {
        unsigned hp[2][2];
#pragma unroll
        for (int jj = 0; jj < 2; ++jj) {
          const int j = 2 * jp + jj;
#pragma unroll
          for (int r = 0; r < 2; ++r) {
            const f32x2 ag = {acc[j][2 * a][2 * r], acc[j][2 * a][2 * r + 1]};
            const f32x2 au = {acc[j][2 * a + 1][2 * r], acc[j][2 * a + 1][2 * r + 1]};
            const f32x2 cgv = {cg4[2 * r], cg4[2 * r + 1]}, cuv = {cu4[2 * r], cu4[2 * r + 1]};
            const f32x2 tg = cgv * rbv[j] + ag;
            const f32x2 tu = cuv * rbv[j] + au;
            const f32x2 z = tg * (tg * tg * k3[j] + k1[j]);
            const f32x2 gu = tg * tu;
            const f32x2 ev = {__builtin_amdgcn_exp2f(z[0]), __builtin_amdgcn_exp2f(z[1])};
            const f32x2 dn = ev * ir2[j] + ir2[j];
            const float h0 = gu[0] * __builtin_amdgcn_rcpf(dn[0]);
            const float h1 = gu[1] * __builtin_amdgcn_rcpf(dn[1]);
            hp[jj][r] = pk2(h0, h1);
          }
        }
        auto s0 = __builtin_amdgcn_permlane16_swap(hp[0][0], hp[1][0], false, false);
        auto s1 = __builtin_amdgcn_permlane16_swap(hp[0][1], hp[1][1], false, false);
        bst128(a2r, so[jp] + a * 8192, (u32x4){s0[0], s1[0], s0[1], s1[1]});
      }
    }
  } else {
#pragma unroll
    for (int n = 0; n < NT; ++n) {
      const int tau = (n >> 2) * 8 + wn * 4 + (n & 3);
      const f32x4 c4 = *(LDS_AS f32x4*)(csum + (wn * NT + n) * 16 + 4 * g);
      if (tau < 15) {
#pragma unroll
        for (int j = 0; j < MT; ++j) {
          const f32x4 v = acc[j][n] * ra[j] + c4 * rbv[j];
          *(LDS_AS u32x2*)(uintptr_t)(lds0 + lrow[j] * QRS + tau * 32 + g * 8) = (u32x2){pk2(v[0], v[1]), pk2(v[2], v[3])};
        }
      }
    }
    lds_barrier();
    STAMP(4);
    const float sc = 0.22360679775f * 1.44269504089f;
    constexpr int NI = 6;
    const int h = w & 3, mt0 = 1 + (w >> 2);
    const bool odd = m & 1;
    const bool lea = 4 * g + (m & 1) <= m, leb = 4 * g + 2 + (m & 1) <= m;
    const unsigned mpar = odd ? 0xffff0000u : 0x0000ffffu;
    const unsigned m0a = lea ? 0u : mpar, m1a = lea ? mpar : 0u, m0b = leb ? 0u : mpar, m1b = leb ? mpar : 0u;
    const unsigned qma = g == 3 ? 0u : ~0u, qmb = g >= 2 ? 0u : ~0u;
    const unsigned rq = lds0 + ((mt0 - 1) * 16 + m) * QRS + h * 40 + g * 16;
    const unsigned va = lds0 + ((mt0 - 1) * 16 + 4 * g + q) * QRS + 160 + h * 40 + p * 8;
    const int kq = cg * 80 + h * 20;
    const unsigned vo0 = a2_off(m, kq + 4 * g), vo1 = g == 0 ? a2_off(m, kq + 16) : OOB;
    bf8 v0[NI], v1[NI];
    f32x4 s0[NI], s1[NI];
    const f32x4 z4 = {0.f, 0.f, 0.f, 0.f};
#pragma unroll
    for (int ii = 0; ii < NI; ++ii) {
      const unsigned r0 = rq + ii * 32 * QRS, vv = va + ii * 32 * QRS;
      const u32x2 k0a = lds_b64(r0), k0b = lds_b64(r0 + 8);
      const u32x2 k1a = lds_b64(r0 + 16 * QRS), k1b = lds_b64(r0 + 16 * QRS + 8);
      const u32x2 qa = lds_b64(r0 + 16 * QRS + 320), qb = lds_b64(r0 + 16 * QRS + 328);
      v0[ii] = cat8(tr4(vv), tr4(vv + 16 * QRS));
      v1[ii] = cat8(tr4(vv + 32), tr4(vv + 16 * QRS + 32));
      const bf8 qf = __builtin_bit_cast(bf8, (u32x4){qa[0] & qma, qa[1] & qma, qb[0] & qmb, qb[1] & qmb});
      s0[ii] = mfma(__builtin_bit_cast(bf8, (u32x4){k0a[0], k0a[1], k0b[0], k0b[1]}), qf, z4);
      s1[ii] = mfma(__builtin_bit_cast(bf8, (u32x4){k1a[0], k1a[1], k1b[0], k1b[1]}), qf, z4);
    }
    u32x2 oa[NI], ob[NI];
#pragma unroll
    for (int ii = 0; ii < NI; ++ii) {
      float la = lea ? (odd ? s1[ii][1] : s1[ii][0]) : (odd ? s0[ii][1] : s0[ii][0]);
      float lb = leb ? (odd ? s1[ii][3] : s1[ii][2]) : (odd ? s0[ii][3] : s0[ii][2]);
      if (ii == 0) {
        const bool bad = rbase + (mt0 - 1) * 16 < 0;
        la = (bad && !lea) ? -3.0e38f : la;
        lb = (bad && !leb) ? -3.0e38f : lb;
      }
      const float mx = red_max(fmaxf(la, lb)) * sc;
      const float ea = __builtin_amdgcn_exp2f(la * sc - mx), eb = __builtin_amdgcn_exp2f(lb * sc - mx);
      const float inv = __builtin_amdgcn_rcpf(red_sum(ea + eb));
      const unsigned pa = pk2(ea * inv, ea * inv), pb = pk2(eb * inv, eb * inv);
      const bf8 pf = __builtin_bit_cast(bf8, (u32x4){pa & m0a, pb & m0b, pa & m1a, pb & m1b});
      const f32x4 o0 = mfma(v0[ii], pf, z4);
      const f32x4 o1 = mfma(v1[ii], pf, z4);
      oa[ii] = (u32x2){pk2(o0[0], o0[1]), pk2(o0[2], o0[3])};
      ob[ii] = (u32x2){pk2(o1[0], o1[1]), pk2(o1[2], o1[3])};
    }
#pragma unroll
    for (int ii = 0; ii < NI; ++ii) {
      const int mt = mt0 + 2 * ii, T = rb * 11 - 1 + mt;
      if (mt < 12 && T < 128) {
        const int so = (T >> 2) * (KS2 * 4096) + (T & 3) * 1024;
        __builtin_amdgcn_raw_buffer_store_b64(oa[ii], a2r, vo0, so, ST_AUX);
        __builtin_amdgcn_raw_buffer_store_b64(ob[ii], a2r, vo1, so, ST_AUX);
      }
    }
  }
  STAMP(5);
}

__global__ __launch_bounds__(512, 2) void phase1(const char* __restrict__ x, const char* __restrict__ wqkv,
                                                 const char* __restrict__ wo, const char* __restrict__ wg,
                                                 const char* __restrict__ wu, const char* __restrict__ wd,
                                                 char* __restrict__ A2, char* __restrict__ B2) {
  __shared__ __attribute__((aligned(1024))) char smem[LDS1_BYTES];
  const unsigned lds0 = (unsigned)(uintptr_t)(LDS_AS char*)smem;
  const int b = blockIdx.x, xcd = b & 7, slot = b >> 3;
  if (slot < 12) {
    const int hg = 2 * (xcd & 3) + slot / 6, rb = 6 * (xcd >> 2) + slot % 6;
    phase1_body<1>(x, wqkv, wqkv, wo, wd, A2, B2, rb, hg, lds0, (xcd * 12 + slot) * 8);
  } else {
    const int L = xcd * 20 + (slot - 12);
    if (L >= 153) return;
    int rb, cg;
    if (L < 72) { cg = L >> 3; rb = L & 7; } else { cg = (L - 72) / 9; rb = 8 + (L - 72) % 9; }
    phase1_body<0>(x, wg, wu, wo, wd, A2, B2, rb, cg, lds0, (96 + L) * 8);
  }
}

__global__ __launch_bounds__(256, 1) void phase2(const char* __restrict__ A2, const char* __restrict__ B2,
                                                 const char* __restrict__ x, char* __restrict__ out) {
  __shared__ __attribute__((aligned(16))) f32x4 red[4][4][5][64];
  const int tid = threadIdx.x, lane = tid & 63;
  const int w = __builtin_amdgcn_readfirstlane(tid >> 6);
  const int g = lane >> 4, m = lane & 15;
  const int b = blockIdx.x, xcd = b & 7, slot = b >> 3;
  const int rb = (xcd >> 1) * 8 + (slot & 7), cg = (xcd & 1) * 4 + (slot >> 3);
  const char* ap = A2 + (size_t)(rb * KS2) * 4096;
  const char* bp = B2 + (size_t)(cg * KS2) * 5120;
  const unsigned lo = lane * 16;
  STAMP2(0);
  constexpr int DEP = P2DEP, NI = 19;
  u32x4 af[DEP][4], bfr[DEP][5];
  f32x4 acc[4][5];
#pragma unroll
  for (int i = 0; i < 4; ++i)
#pragma unroll
    for (int n = 0; n < 5; ++n) acc[i][n] = (f32x4){0.f, 0.f, 0.f, 0.f};
  auto load1 = [&](int i, int u) {
    const int ks = w + 4 * ((i == NI - 1 && w >= 2) ? i - 1 : i);
    if (u < 5) bfr[i % DEP][u] = *(const u32x4*)(bp + ks * 5120 + (lo + u * 1024));
    else af[i % DEP][u - 5] = *(const u32x4*)(ap + ks * 4096 + (lo + (u - 5) * 1024));
  };
  const int orow = rb * 64 + w * 16 + m;
  const size_t ooff = (size_t)orow * 1280 + (cg * 80 + 4 * g) * 2;
  u32x2 xr[5];
#pragma unroll
  for (int i = 0; i < DEP - 1; ++i)
#pragma unroll
    for (int u = 0; u < 9; ++u) load1(i, u);
#pragma unroll
  for (int n = 0; n < 5; ++n) xr[n] = *(const u32x2*)(x + ooff + n * 32);
  SB0;
#pragma unroll
  for (int i = 0; i < NI; ++i) {
    if (i < NI - 1 || w < 2) {
#pragma unroll
      for (int u = 0; u < 20; ++u) {
        const int j = u / 5, n = u % 5;
        acc[j][n] = mfma(__builtin_bit_cast(bf8, bfr[i % DEP][n]), __builtin_bit_cast(bf8, af[i % DEP][j]), acc[j][n]);
        if ((u & 1) == 0 && u < 18 && i + DEP - 1 < NI) load1(i + DEP - 1, u >> 1);
        SB0;
      }
    }
  }
  STAMP2(1);
#pragma unroll
  for (int j = 0; j < 4; ++j)
#pragma unroll
    for (int n = 0; n < 5; ++n) red[w][j][n][lane] = acc[j][n];
  __syncthreads();
#pragma unroll
  for (int n = 0; n < 5; ++n) {
    f32x4 s = red[0][w][n][lane];
    s += red[1][w][n][lane];
    s += red[2][w][n][lane];
    s += red[3][w][n][lane];
    const float x0 = __builtin_bit_cast(float, xr[n][0] << 16), x1 = __builtin_bit_cast(float, xr[n][0] & 0xffff0000u);
    const float x2 = __builtin_bit_cast(float, xr[n][1] << 16), x3 = __builtin_bit_cast(float, xr[n][1] & 0xffff0000u);
    *(u32x2*)(out + ooff + n * 32) = (u32x2){pk2(s[0] + x0, s[1] + x1), pk2(s[2] + x2, s[3] + x3)};
  }
  STAMP2(2);
}

extern "C" void kernel_launch(void* const* d_in, const int* in_sizes, int n_in, void* d_out, int out_size, void* d_ws,
                              size_t ws_size, hipStream_t stream) {
  const char* x = (const char*)d_in[0];
  const char* wqkv = (const char*)d_in[1];
  const char* wo = (const char*)d_in[2];
  const char* wg = (const char*)d_in[4];
  const char* wu = (const char*)d_in[5];
  const char* wd = (const char*)d_in[6];
  char* A2 = (char*)d_ws;
  char* B2 = A2 + A2_BYTES;
  phase1<<<256, 512, 0, stream>>>(x, wqkv, wo, wg, wu, wd, A2, B2);
  phase2<<<256, 256, 0, stream>>>(A2, B2, x, (char*)d_out);
}
```

```cpp
#include <hip/hip_runtime.h>
#include <hip/hip_bf16.h>
#include <stdint.h>

typedef __attribute__((ext_vector_type(4))) short s16x4;
typedef __attribute__((ext_vector_type(8))) short s16x8;
typedef __attribute__((ext_vector_type(2))) __bf16 bf2;
typedef __attribute__((ext_vector_type(8))) __bf16 bf8;
typedef __attribute__((ext_vector_type(4))) float f32x4;
typedef __attribute__((ext_vector_type(4))) unsigned u32x4;
typedef __attribute__((ext_vector_type(2))) unsigned u32x2;
#define LDS_AS __attribute__((address_space(3)))
#define DEVI __device__ __forceinline__

constexpr int S_ = 2048, D_ = 640, F_ = 1728;
constexpr int KS2 = 74;
constexpr size_t A2_BYTES = (size_t)32 * KS2 * 4096;
constexpr int STAT_OFF = 147456;
constexpr int LDS1_BYTES = STAT_OFF + 3328;
constexpr int QRS = 488;

#define STAMP(i)
#define STAMP2(i)
template <int ROLE> struct Cfg;
template <> struct Cfg<0> {
  static constexpr int WM = 2, WN = 4, MT = 4, NT = 6, WROW = 384, WSTAGE = 24576, XTILE = 16384,
                       XOFF = 98304, WI = 3, XE = 1, XO = 1;
};
template <> struct Cfg<1> {
  static constexpr int WM = 4, WN = 2, MT = 3, NT = 8, WROW = 512, WSTAGE = 16384, XTILE = 24576,
                       XOFF = 65536, WI = 2, XE = 2, XO = 1;
};
typedef __attribute__((ext_vector_type(2))) float f32x2;

DEVI void dma16(const char* sbase, unsigned voff, unsigned lds_off) {
  asm volatile("s_mov_b32 m0, %0\n\ts_nop 0\n\tglobal_load_lds_dwordx4 %1, %2" ::"s"(lds_off), "v"(voff), "s"(sbase) : "memory");
}
DEVI void dma16m(const char* sbase, unsigned voff, unsigned lds_off, unsigned long long mask) {
  asm volatile("s_mov_b32 m0, %0\n\ts_mov_b64 exec, %3\n\tglobal_load_lds_dwordx4 %1, %2\n\ts_mov_b64 exec, -1"
               ::"s"(lds_off), "v"(voff), "s"(sbase), "s"(mask) : "memory");
}
#define SB0 __builtin_amdgcn_sched_barrier(0)
template <int N> DEVI void wait_vm_lgkm0() {
  asm volatile("" ::: "memory");
  __builtin_amdgcn_s_waitcnt((N & 15) | ((N >> 4) << 14) | (7 << 4));
  asm volatile("" ::: "memory");
}
DEVI void lds_barrier() {
  asm volatile("" ::: "memory");
  __builtin_amdgcn_s_waitcnt(0xC07F);
  __builtin_amdgcn_s_barrier();
  asm volatile("" ::: "memory");
}
DEVI s16x4 tr4(unsigned a) { return __builtin_amdgcn_ds_read_tr16_b64_v4i16((LDS_AS s16x4*)(uintptr_t)a); }
DEVI bf8 cat8(s16x4 lo, s16x4 hi) { return __builtin_bit_cast(bf8, __builtin_shufflevector(lo, hi, 0, 1, 2, 3, 4, 5, 6, 7)); }
DEVI bf8 lds_b128(unsigned a) { return __builtin_bit_cast(bf8, *(LDS_AS u32x4*)(uintptr_t)a); }
DEVI u32x2 lds_b64(unsigned a) { return *(LDS_AS u32x2*)(uintptr_t)a; }
DEVI unsigned pk2(float a, float b) {
  bf2 v = {(__bf16)a, (__bf16)b};
  return __builtin_bit_cast(unsigned, v);
}
DEVI f32x4 mfma(bf8 a, bf8 b, f32x4 c) { return __builtin_amdgcn_mfma_f32_16x16x32_bf16(a, b, c, 0, 0, 0); }
DEVI float fbits(unsigned u) { return __builtin_bit_cast(float, u); }
DEVI unsigned ubits(float f) { return __builtin_bit_cast(unsigned, f); }
#define P2DEP 4
#define ST_AUX 16
typedef __amdgpu_buffer_rsrc_t rsrc_t;
DEVI rsrc_t mk_rsrc(void* p, unsigned bytes) { return __builtin_amdgcn_make_buffer_rsrc(p, 0, bytes, 0x00020000); }
DEVI void bst64(rsrc_t r, unsigned off, u32x2 v) { __builtin_amdgcn_raw_buffer_store_b64(v, r, off, 0, ST_AUX); }
DEVI void bst128(rsrc_t r, unsigned off, u32x4 v) { __builtin_amdgcn_raw_buffer_store_b128(v, r, off, 0, ST_AUX); }
constexpr unsigned OOB = 0x80000000u;
DEVI float vmax(float a, float b) { float r; asm("v_max_f32 %0, %1, %2" : "=v"(r) : "v"(a), "v"(b)); return r; }
DEVI float red_max(float v) {
  auto a = __builtin_amdgcn_permlane16_swap(ubits(v), ubits(v), false, false);
  v = vmax(fbits(a[0]), fbits(a[1]));
  auto b = __builtin_amdgcn_permlane32_swap(ubits(v), ubits(v), false, false);
  return vmax(fbits(b[0]), fbits(b[1]));
}
DEVI float red_sum(float v) {
  auto a = __builtin_amdgcn_permlane16_swap(ubits(v), ubits(v), false, false);
  v = fbits(a[0]) + fbits(a[1]);
  auto b = __builtin_amdgcn_permlane32_swap(ubits(v), ubits(v), false, false);
  return fbits(b[0]) + fbits(b[1]);
}
DEVI unsigned a2_off(int r, int k) {
  return (unsigned)(((r >> 6) * KS2 + (k >> 5)) * 4096 + ((r >> 4) & 3) * 1024 + ((((k >> 3) & 3) * 16 + (r & 15)) * 16) +
                    ((k >> 2) & 1) * 8);
}
template <int ROLE> constexpr int n_issue(int t) {
  return (t <= 15 ? Cfg<ROLE>::WI : 0) + (t <= 14 ? (((t + 5) & 1) ? Cfg<ROLE>::XO : Cfg<ROLE>::XE) : 0);
}

template <int ROLE>
DEVI void phase1_body(const char* __restrict__ x, const char* __restrict__ wa, const char* __restrict__ wb,
                      const char* __restrict__ wo, const char* __restrict__ wd, char* __restrict__ A2,
                      char* __restrict__ B2, int rb, int cg, unsigned lds0, int job) {
  using C = Cfg<ROLE>;
  constexpr int MT = C::MT, NT = C::NT;
  const int tid = threadIdx.x, lane = tid & 63;
  const int w = __builtin_amdgcn_readfirstlane(tid >> 6);
  const int g = lane >> 4, m = lane & 15, q = m >> 2, p = lane & 3;
  const int wm = w / C::WN, wn = w % C::WN;
  const int rbase = ROLE == 0 ? rb * 127 - 1 : rb * 176 - 16;
  STAMP(0);

  unsigned wof[C::WI];
  unsigned long long wmask[C::WI];
  constexpr int WSTRIDE = ROLE == 0 ? F_ * 2 : 3 * D_ * 2;
  const char* wbase;
  if (ROLE == 0) {
    wbase = (w < 4 ? wa : wb) + cg * 384;
#pragma unroll
    for (int i = 0; i < C::WI; ++i) {
      const int idx = ((w & 3) * 3 + i) * 64 + lane;
      const int row = idx / 24, pc = idx % 24;
      const int f = ((row >> 1) & 1) | ((row >> 2) & 2);
      wof[i] = row * WSTRIDE + (((pc >> 1) ^ f) * 32) + (pc & 1) * 16;
    }
  } else {
    wbase = wa + cg * 160;
#pragma unroll
    for (int i = 0; i < C::WI; ++i) {
      const int row = 4 * w + 2 * i + (lane >> 5), pc = lane & 31;
      const int f = (row & 3) | ((row >> 1) & 4);
      const int c = 2 * ((pc >> 1) ^ f) + (pc & 1);
      const int part = c / 10, cc = c % 10;
      const int poff = part == 0 ? 1280 : (part == 1 ? 2560 : 0);
      wof[i] = row * WSTRIDE + poff + cc * 16;
      wmask[i] = __ballot(c < 30);
    }
  }
  constexpr int XI = C::XE + C::XO;
  unsigned xof[XI];
#pragma unroll
  for (int i = 0; i < XI; ++i) {
    const int lr = (i * 8 + w) * 8 + (lane >> 3);
    int gr = rbase + lr;
    gr = gr < 0 ? 0 : (gr > S_ - 1 ? S_ - 1 : gr);
    xof[i] = gr * 1280 + ((lane & 7) ^ ((lr >> 1) & 7)) * 16;
  }
  const unsigned long long xmask0 = w == 0 ? ~0xffull : ~0ull;
  const unsigned wdst = lds0 + w * (C::WI * 1024);
  const unsigned xdst = lds0 + C::XOFF + w * 1024;
  auto dma_w = [&](int d, int stage) {
    const char* b = wbase + (size_t)stage * (32 * WSTRIDE);
    if (ROLE == 0) dma16(b, wof[d], wdst + (stage & 3) * C::WSTAGE + d * 1024);
    else dma16m(b, wof[d], wdst + (stage & 3) * C::WSTAGE + d * 1024, wmask[d]);
  };
  auto dma_x = [&](int i, int tile, int buf) {
    if (i == 0) dma16m(x + tile * 128, xof[i], xdst + buf * C::XTILE, xmask0);
    else dma16(x + tile * 128, xof[i], xdst + buf * C::XTILE + i * 8192);
  };
  auto issue_w = [&](int stage) {
#pragma unroll
    for (int i = 0; i < C::WI; ++i) dma_w(i, stage);
  };
  auto issue_x = [&](int odd, int tile) {
#pragma unroll
    for (int i = (odd ? C::XE : 0); i < (odd ? XI : C::XE); ++i) dma_x(i, tile, tile);
  };
  issue_x(0, 0);
  issue_w(0); issue_x(1, 0);
  issue_w(1); issue_x(0, 1);
  const u32x4 ones4 = {0x3F803F80u, 0x3F803F80u, 0x3F803F80u, 0x3F803F80u};
  if (tid < 24) *(LDS_AS u32x4*)(uintptr_t)(lds0 + C::XOFF + (tid >> 3) * C::XTILE + (tid & 7) * 16) = ones4;
  if (ROLE == 1 && tid >= 256) {
    const int t2 = tid - 256, row = (t2 >> 1) & 31;
    const int f = (row & 3) | ((row >> 1) & 4);
    *(LDS_AS u32x4*)(uintptr_t)(lds0 + (t2 >> 6) * C::WSTAGE + row * 512 + (15 ^ f) * 32 + (t2 & 1) * 16) = ones4;
  }

  constexpr int NWA = ROLE == 0 ? 1 : 4;
  unsigned wad[NWA], xad[MT];
  if (ROLE == 0) {
    const int f = ((q >> 1) & 1) | ((g & 1) << 1);
    wad[0] = lds0 + (8 * g + q) * 384 + ((wn ^ f) * 32) + p * 8;
  } else {
    const int f = q | ((g & 1) << 2);
#pragma unroll
    for (int e = 0; e < NWA; ++e) wad[e] = lds0 + (8 * g + q) * 512 + (((wn * 4 + e) ^ f) * 32) + p * 8;
  }
  int lrow[MT];
#pragma unroll
  for (int j = 0; j < MT; ++j) {
    const int act = ROLE == 0 ? ((j + wn) & 3) : (wn ? 2 - j : j);
    lrow[j] = (wm * MT + act) * 16 + m;
    xad[j] = lds0 + C::XOFF + lrow[j] * 128 + ((g ^ ((lrow[j] >> 1) & 7)) * 16);
  }
  f32x4 acc[MT][NT];
#pragma unroll
  for (int j = 0; j < MT; ++j)
#pragma unroll
    for (int n = 0; n < NT; ++n) acc[j][n] = (f32x4){0.f, 0.f, 0.f, 0.f};
  f32x4 st0 = {0.f, 0.f, 0.f, 0.f}, st1 = {0.f, 0.f, 0.f, 0.f};
  const bf8 onesf = __builtin_bit_cast(bf8, ones4);
  bf8 xf[2][MT];
  s16x4 wh[2][NT][2];

  auto read_one = [&](int par, int r, unsigned woff, unsigned xoff) {
    if (r < MT) {
      xf[par][r] = lds_b128((xad[r] + xoff) ^ (par ? 64u : 0u));
    } else {
      const int n = (r - MT) >> 1, hl = (r - MT) & 1;
      if (ROLE == 0) wh[par][n][hl] = tr4(wad[0] + woff + (n & 1) * 12288 + (n >> 1) * 128 + hl * 4 * 384);
      else wh[par][n][hl] = tr4(wad[n & 3] + woff + (n >> 2) * 256 + hl * 4 * 512);
    }
  };
  constexpr int NR = MT + 2 * NT;

  wait_vm_lgkm0<n_issue<ROLE>(-3)>();
  __builtin_amdgcn_s_barrier();
#pragma unroll
  for (int r = 0; r < NR; ++r) read_one(0, r, 0, 0);
  STAMP(1);

  int xr = 0;
#define JOB_HOOK
#define ITER(T, PAR, VM, DOW, DOX, DORD, DBL)                                                    \
  {                                                                                          \
    if (DORD) { wait_vm_lgkm0<VM>(); __builtin_amdgcn_s_barrier(); }                         \
    SB0;                                                                                     \
    const int xn = xr == 2 ? 0 : xr + 1;                                                     \
    const int xq = xn == 2 ? 0 : xn + 1;                                                     \
    const unsigned woff = (((T) + 1) & 3) * C::WSTAGE, xoff = (PAR ? xn : xr) * C::XTILE;    \
    _Pragma("unroll") for (int i = 0; i < 24; ++i) {                                         \
      const int n = i / MT, j = i % MT;                                                      \
      acc[j][n] = mfma(cat8(wh[PAR][n][0], wh[PAR][n][1]), xf[PAR][j], acc[j][n]);           \
      if (DORD && i < 20 && i * NR / 20 != (i + 1) * NR / 20) read_one(PAR ^ 1, i * NR / 20, woff, xoff); \
      if (DBL && i % 3 == 2) {     \
        const int V = 2 * (PAR) - 2 + (i / 12), d = (i / 3) & 3, pc_ = V + 5;                \
        if (d < C::WI) dma_w(d, V + 4);                                                      \
        else {                                                                               \
          const int xi = ((pc_ & 1) ? C::XE : 0) + (d - C::WI);                              \
          if (xi < ((pc_ & 1) ? XI : C::XE)) dma_x(xi, pc_ >> 1, (pc_ >> 1) % 3);            \
        }                                                                                    \
      }                                                                                      \
      if (!DBL && DOW && i % 6 == 2) {                                                       \
        const int d = i / 6;                                                                 \
        if (d < C::WI) dma_w(d, (T) + 4);                                                    \
        else if (DOX) {                                                                      \
          const int xi = (PAR ? 0 : C::XE) + (d - C::WI);                                    \
          if (xi < (PAR ? C::XE : XI)) dma_x(xi, ((T) + 5) >> 1, PAR ? xr : xq);             \
        }                                                                                    \
      }                                                                                      \
      JOB_HOOK                                                                               \
      if (i == 21) st0 = mfma(xf[PAR][0], xf[PAR][0], st0);                                  \
      if (i == 22) {                                                                         \
        if (ROLE == 0) st1 = mfma(onesf, xf[PAR][0], st1);                                   \
        else if (wn == PAR) st1 = mfma(xf[PAR][1], xf[PAR][1], st1);                         \
      }                                                                                      \
      SB0;                                                                                   \
    }                                                                                        \
    if (PAR) xr = xn;                                                                        \
  }
  constexpr int VMF = n_issue<ROLE>(0) + n_issue<ROLE>(1);
  ITER(0, 0, 0, true, true, true, true)
  ITER(1, 1, n_issue<ROLE>(-1), true, true, true, true)
  for (int t = 2; t < 14; t += 2) {
    ITER(t, 0, VMF, true, true, true, false)
    ITER(t + 1, 1, VMF, true, true, true, false)
  }
  ITER(14, 0, VMF, true, true, true, false)
  ITER(15, 1, VMF, true, false, true, false)
  ITER(16, 0, n_issue<ROLE>(14) + n_issue<ROLE>(15), false, false, true, false)
  ITER(17, 1, n_issue<ROLE>(15), false, false, true, false)
  unsigned jl[8];
  const int J = job + w < 1480 ? job + w : job + w - 1480;
  const int jks = J / 20, jcb = J % 20;
  const char* jsrc = (jks < 20 ? wo + (size_t)(jks * 32) * 1280 : wd + (size_t)(jks * 32 - 640) * 1280) + jcb * 64;
#undef JOB_HOOK
#define JOB_HOOK if (i < 8) jl[i] = *(const unsigned*)(jsrc + (unsigned)((8 * g + i) * 1280 + m * 4));
  ITER(18, 0, 0, false, false, true, false)
#undef JOB_HOOK
#define JOB_HOOK
  ITER(19, 1, 0, false, false, false, false)
#undef ITER
#undef JOB_HOOK
  STAMP(2);

  LDS_AS float* rsum = (LDS_AS float*)(uintptr_t)(lds0 + STAT_OFF);
  LDS_AS float* rsq = rsum + 192;
  LDS_AS float* csum = rsum + 448;
  {
    const bool diag = g == q;
    const unsigned e0 = p == 0 ? ~0u : 0u, e1 = p == 1 ? ~0u : 0u, e2 = p == 2 ? ~0u : 0u, e3 = p == 3 ? ~0u : 0u;
    const float d0 = fbits((ubits(st0[0]) & e0) | (ubits(st0[1]) & e1) | (ubits(st0[2]) & e2) | (ubits(st0[3]) & e3));
    if (ROLE == 0) {
      if (diag) { rsq[lrow[0]] = d0; rsum[lrow[0]] = st1[0]; }
    } else {
      const float d1 = fbits((ubits(st1[0]) & e0) | (ubits(st1[1]) & e1) | (ubits(st1[2]) & e2) | (ubits(st1[3]) & e3));
      if (diag) {
        rsq[lrow[0]] = d0;
        rsq[wn ? 192 + wm * 16 + m : lrow[1]] = d1;
        if (wn) {
#pragma unroll
          for (int j = 0; j < MT; ++j) rsum[lrow[j]] = acc[j][7][0];
        }
      }
    }
  }
  if (wm == 0) {
#pragma unroll
    for (int j = 0; j < MT; ++j) {
      if (lrow[j] == 0) {
#pragma unroll
        for (int n = 0; n < NT; ++n) *(LDS_AS f32x4*)(csum + (wn * NT + n) * 16 + 4 * g) = acc[j][n];
      }
    }
  }
  lds_barrier();
  float ra[MT], rbv[MT], ir2[MT], k1[MT], k3[MT];
#pragma unroll
  for (int j = 0; j < MT; ++j) {
    const float mean = rsum[lrow[j]] * (1.f / D_);
    float sq = rsq[lrow[j]];
    if (ROLE == 1 && j == 1) sq += rsq[192 + wm * 16 + m];
    const float var = fmaxf(sq * (1.f / D_) - mean * mean, 0.f);
    ra[j] = __builtin_amdgcn_rsqf(var + 1e-5f);
    rbv[j] = ROLE == 0 ? -mean : -mean * ra[j];
    ir2[j] = var + 1e-5f;
    k1[j] = -2.3022082f * ra[j];
    k3[j] = -0.10294324f * ra[j] * ra[j] * ra[j];
  }
  STAMP(3);
  {
    const int n0 = jcb * 32 + 2 * m, nt = n0 >> 4;
    const rsrc_t b2r = mk_rsrc(B2, 8 * KS2 * 5120);
    const unsigned dst = job + w < 1480 ? (unsigned)((((nt / 5) * KS2 + jks) * 5 + nt % 5) * 1024 + (g * 16 + (n0 & 15)) * 16) : OOB;
#pragma unroll
    for (int i = 0; i < 2; ++i) {
      u32x4 o;
#pragma unroll
      for (int j = 0; j < 4; ++j) o[j] = __builtin_amdgcn_perm(jl[2 * j + 1], jl[2 * j], i ? 0x07060302u : 0x05040100u);
      bst128(b2r, dst + i * 16, o);
    }
  }
  const rsrc_t a2r = mk_rsrc(A2, (unsigned)A2_BYTES);
  if (ROLE == 0) {
    unsigned so[2];
#pragma unroll
    for (int jp = 0; jp < 2; ++jp) {
      const int lr = (wm * 4 + ((2 * jp + (g & 1) + wn) & 3)) * 16 + m;
      const int gr = rbase + lr;
      so[jp] = (lr >= 1 && gr < S_) ? a2_off(gr, 640 + cg * 192 + wn * 16 + (g >> 1) * 8) : OOB;
    }
#pragma unroll
    for (int a = 0; a < 3; ++a) {
      const f32x4 cg4 = *(LDS_AS f32x4*)(csum + (wn * NT + 2 * a) * 16 + 4 * g);
      const f32x4 cu4 = *(LDS_AS f32x4*)(csum + (wn * NT + 2 * a + 1) * 16 + 4 * g);
#pragma unroll
      for (int jp = 0; jp < 2; ++jp) {
        unsigned hp[2][2];
#pragma unroll
        for (int jj = 0; jj < 2; ++jj) {
          const int j = 2 * jp + jj;
#pragma unroll
          for (int r = 0; r < 2; ++r) {
            const f32x2 ag = {acc[j][2 * a][2 * r], acc[j][2 * a][2 * r + 1]};
            const f32x2 au = {acc[j][2 * a + 1][2 * r], acc[j][2 * a + 1][2 * r + 1]};
            const f32x2 cgv = {cg4[2 * r], cg4[2 * r + 1]}, cuv = {cu4[2 * r], cu4[2 * r + 1]};
            const f32x2 tg = cgv * rbv[j] + ag;
            const f32x2 tu = cuv * rbv[j] + au;
            const f32x2 z = tg * (tg * tg * k3[j] + k1[j]);
            const f32x2 gu = tg * tu;
            const f32x2 ev = {__builtin_amdgcn_exp2f(z[0]), __builtin_amdgcn_exp2f(z[1])};
            const f32x2 dn = ev * ir2[j] + ir2[j];
            const float h0 = gu[0] * __builtin_amdgcn_rcpf(dn[0]);
            const float h1 = gu[1] * __builtin_amdgcn_rcpf(dn[1]);
            hp[jj][r] = pk2(h0, h1);
          }
        }
        auto s0 = __builtin_amdgcn_permlane16_swap(hp[0][0], hp[1][0], false, false);
        auto s1 = __builtin_amdgcn_permlane16_swap(hp[0][1], hp[1][1], false, false);
        bst128(a2r, so[jp] + a * 8192, (u32x4){s0[0], s1[0], s0[1], s1[1]});
      }
    }
  } else {
#pragma unroll
    for (int n = 0; n < NT; ++n) {
      const int tau = (n >> 2) * 8 + wn * 4 + (n & 3);
      const f32x4 c4 = *(LDS_AS f32x4*)(csum + (wn * NT + n) * 16 + 4 * g);
      if (tau < 15) {
#pragma unroll
        for (int j = 0; j < MT; ++j) {
          const f32x4 v = acc[j][n] * ra[j] + c4 * rbv[j];
          *(LDS_AS u32x2*)(uintptr_t)(lds0 + lrow[j] * QRS + tau * 32 + g * 8) = (u32x2){pk2(v[0], v[1]), pk2(v[2], v[3])};
        }
      }
    }
    lds_barrier();
    STAMP(4);
    const float sc = 0.22360679775f * 1.44269504089f;
    constexpr int NI = 6;
    const int h = w & 3, mt0 = 1 + (w >> 2);
    const bool odd = m & 1;
    const bool lea = 4 * g + (m & 1) <= m, leb = 4 * g + 2 + (m & 1) <= m;
    const unsigned mpar = odd ? 0xffff0000u : 0x0000ffffu;
    const unsigned m0a = lea ? 0u : mpar, m1a = lea ? mpar : 0u, m0b = leb ? 0u : mpar, m1b = leb ? mpar : 0u;
    const unsigned qma = g == 3 ? 0u : ~0u, qmb = g >= 2 ? 0u : ~0u;
    const unsigned rq = lds0 + ((mt0 - 1) * 16 + m) * QRS + h * 40 + g * 16;
    const unsigned va = lds0 + ((mt0 - 1) * 16 + 4 * g + q) * QRS + 160 + h * 40 + p * 8;
    const int kq = cg * 80 + h * 20;
    const unsigned vo0 = a2_off(m, kq + 4 * g), vo1 = g == 0 ? a2_off(m, kq + 16) : OOB;
    bf8 v0[NI], v1[NI];
    f32x4 s0[NI], s1[NI];
    const f32x4 z4 = {0.f, 0.f, 0.f, 0.f};
#pragma unroll
    for (int ii = 0; ii < NI; ++ii) {
      const unsigned r0 = rq + ii * 32 * QRS, vv = va + ii * 32 * QRS;
      const u32x2 k0a = lds_b64(r0), k0b = lds_b64(r0 + 8);
      const u32x2 k1a = lds_b64(r0 + 16 * QRS), k1b = lds_b64(r0 + 16 * QRS + 8);
      const u32x2 qa = lds_b64(r0 + 16 * QRS + 320), qb = lds_b64(r0 + 16 * QRS + 328);
      v0[ii] = cat8(tr4(vv), tr4(vv + 16 * QRS));
      v1[ii] = cat8(tr4(vv + 32), tr4(vv + 16 * QRS + 32));
      const bf8 qf = __builtin_bit_cast(bf8, (u32x4){qa[0] & qma, qa[1] & qma, qb[0] & qmb, qb[1] & qmb});
      s0[ii] = mfma(__builtin_bit_cast(bf8, (u32x4){k0a[0], k0a[1], k0b[0], k0b[1]}), qf, z4);
      s1[ii] = mfma(__builtin_bit_cast(bf8, (u32x4){k1a[0], k1a[1], k1b[0], k1b[1]}), qf, z4);
    }
    u32x2 oa[NI], ob[NI];
    const unsigned ev = odd ? 0u : ~0u, od = ~ev, sa = lea ? ~0u : 0u, sb = leb ? ~0u : 0u;
    const bool bad = rbase + (mt0 - 1) * 16 < 0;
    const unsigned bada = (bad && !lea) ? ~0u : 0u, badb = (bad && !leb) ? ~0u : 0u;
    float la[NI], lb[NI], mx[NI];
#pragma unroll
    for (int ii = 0; ii < NI; ++ii) {
      unsigned ua = (ubits(s0[ii][0]) & (ev & ~sa)) | (ubits(s0[ii][1]) & (od & ~sa)) | (ubits(s1[ii][0]) & (ev & sa)) | (ubits(s1[ii][1]) & (od & sa));
      unsigned ub = (ubits(s0[ii][2]) & (ev & ~sb)) | (ubits(s0[ii][3]) & (od & ~sb)) | (ubits(s1[ii][2]) & (ev & sb)) | (ubits(s1[ii][3]) & (od & sb));
      if (ii == 0) {
        ua = (ua & ~bada) | (0xff7fffffu & bada);
        ub = (ub & ~badb) | (0xff7fffffu & badb);
      }
      la[ii] = fbits(ua); lb[ii] = fbits(ub);
      mx[ii] = vmax(la[ii], lb[ii]);
    }
#pragma unroll
    for (int ii = 0; ii < NI; ++ii) mx[ii] = red_max(mx[ii]) * sc;
#pragma unroll
    for (int ii = 0; ii < NI; ++ii) {
      la[ii] = __builtin_amdgcn_exp2f(la[ii] * sc - mx[ii]);
      lb[ii] = __builtin_amdgcn_exp2f(lb[ii] * sc - mx[ii]);
      mx[ii] = la[ii] + lb[ii];
    }
#pragma unroll
    for (int ii = 0; ii < NI; ++ii) mx[ii] = __builtin_amdgcn_rcpf(red_sum(mx[ii]));
#pragma unroll
    for (int ii = 0; ii < NI; ++ii) {
      const float pa_ = la[ii] * mx[ii], pb_ = lb[ii] * mx[ii];
      const unsigned pa = pk2(pa_, pa_), pb = pk2(pb_, pb_);
      const bf8 pf = __builtin_bit_cast(bf8, (u32x4){pa & m0a, pb & m0b, pa & m1a, pb & m1b});
      s0[ii] = mfma(v0[ii], pf, z4);
      s1[ii] = mfma(v1[ii], pf, z4);
    }
#pragma unroll
    for (int ii = 0; ii < NI; ++ii) {
      oa[ii] = (u32x2){pk2(s0[ii][0], s0[ii][1]), pk2(s0[ii][2], s0[ii][3])};
      ob[ii] = (u32x2){pk2(s1[ii][0], s1[ii][1]), pk2(s1[ii][2], s1[ii][3])};
    }
#pragma unroll
    for (int ii = 0; ii < NI; ++ii) {
      const int mt = mt0 + 2 * ii, T = rb * 11 - 1 + mt;
      if (mt < 12 && T < 128) {
        const int so = (T >> 2) * (KS2 * 4096) + (T & 3) * 1024;
        __builtin_amdgcn_raw_buffer_store_b64(oa[ii], a2r, vo0, so, ST_AUX);
        __builtin_amdgcn_raw_buffer_store_b64(ob[ii], a2r, vo1, so, ST_AUX);
      }
    }
  }
  STAMP(5);
}

__global__ __launch_bounds__(512, 2) void phase1(const char* __restrict__ x, const char* __restrict__ wqkv,
                                                 const char* __restrict__ wo, const char* __restrict__ wg,
                                                 const char* __restrict__ wu, const char* __restrict__ wd,
                                                 char* __restrict__ A2, char* __restrict__ B2) {
  __shared__ __attribute__((aligned(1024))) char smem[LDS1_BYTES];
  const unsigned lds0 = (unsigned)(uintptr_t)(LDS_AS char*)smem;
  const int b = blockIdx.x, xcd = b & 7, slot = b >> 3;
  if (slot < 12) {
    const int hg = 2 * (xcd & 3) + slot / 6, rb = 6 * (xcd >> 2) + slot % 6;
    phase1_body<1>(x, wqkv, wqkv, wo, wd, A2, B2, rb, hg, lds0, (xcd * 12 + slot) * 8);
  } else {
    const int L = xcd * 20 + (slot - 12);
    if (L >= 153) return;
    int rb, cg;
    if (L < 72) { cg = L >> 3; rb = L & 7; } else { cg = (L - 72) / 9; rb = 8 + (L - 72) % 9; }
    phase1_body<0>(x, wg, wu, wo, wd, A2, B2, rb, cg, lds0, (96 + L) * 8);
  }
}

__global__ __launch_bounds__(256, 1) void phase2(const char* __restrict__ A2, const char* __restrict__ B2,
                                                 const char* __restrict__ x, char* __restrict__ out) {
  __shared__ __attribute__((aligned(16))) f32x4 red[4][4][5][64];
  const int tid = threadIdx.x, lane = tid & 63;
  const int w = __builtin_amdgcn_readfirstlane(tid >> 6);
  const int g = lane >> 4, m = lane & 15;
  const int b = blockIdx.x, xcd = b & 7, slot = b >> 3;
  const int rb = (xcd >> 1) * 8 + (slot & 7), cg = (xcd & 1) * 4 + (slot >> 3);
  const char* ap = A2 + (size_t)(rb * KS2) * 4096;
  const char* bp = B2 + (size_t)(cg * KS2) * 5120;
  const unsigned lo = lane * 16;
  STAMP2(0);
  constexpr int DEP = P2DEP, NI = 19;
  u32x4 af[DEP][4], bfr[DEP][5];
  f32x4 acc[4][5];
#pragma unroll
  for (int i = 0; i < 4; ++i)
#pragma unroll
    for (int n = 0; n < 5; ++n) acc[i][n] = (f32x4){0.f, 0.f, 0.f, 0.f};
  auto load1 = [&](int i, int u) {
    const int ks = w + 4 * ((i == NI - 1 && w >= 2) ? i - 1 : i);
    if (u < 5) bfr[i % DEP][u] = *(const u32x4*)(bp + ks * 5120 + (lo + u * 1024));
    else af[i % DEP][u - 5] = *(const u32x4*)(ap + ks * 4096 + (lo + (u - 5) * 1024));
  };
  const int orow = rb * 64 + w * 16 + m;
  const size_t ooff = (size_t)orow * 1280 + (cg * 80 + 4 * g) * 2;
  u32x2 xr[5];
#pragma unroll
  for (int i = 0; i < DEP - 1; ++i)
#pragma unroll
    for (int u = 0; u < 9; ++u) load1(i, u);
#pragma unroll
  for (int n = 0; n < 5; ++n) xr[n] = *(const u32x2*)(x + ooff + n * 32);
  SB0;
#pragma unroll
  for (int i = 0; i < NI; ++i) {
    if (i < NI - 1 || w < 2) {
#pragma unroll
      for (int u = 0; u < 20; ++u) {
        const int j = u / 5, n = u % 5;
        acc[j][n] = mfma(__builtin_bit_cast(bf8, bfr[i % DEP][n]), __builtin_bit_cast(bf8, af[i % DEP][j]), acc[j][n]);
        if ((u & 1) == 0 && u < 18 && i + DEP - 1 < NI) load1(i + DEP - 1, u >> 1);
        SB0;
      }
    }
  }
  STAMP2(1);
#pragma unroll
  for (int j = 0; j < 4; ++j)
#pragma unroll
    for (int n = 0; n < 5; ++n) red[w][j][n][lane] = acc[j][n];
  __syncthreads();
#pragma unroll
  for (int n = 0; n < 5; ++n) {
    f32x4 s = red[0][w][n][lane];
    s += red[1][w][n][lane];
    s += red[2][w][n][lane];
    s += red[3][w][n][lane];
    const float x0 = __builtin_bit_cast(float, xr[n][0] << 16), x1 = __builtin_bit_cast(float, xr[n][0] & 0xffff0000u);
    const float x2 = __builtin_bit_cast(float, xr[n][1] << 16), x3 = __builtin_bit_cast(float, xr[n][1] & 0xffff0000u);
    *(u32x2*)(out + ooff + n * 32) = (u32x2){pk2(s[0] + x0, s[1] + x1), pk2(s[2] + x2, s[3] + x3)};
  }
  STAMP2(2);
}

extern "C" void kernel_launch(void* const* d_in, const int* in_sizes, int n_in, void* d_out, int out_size, void* d_ws,
                              size_t ws_size, hipStream_t stream) {
  const char* x = (const char*)d_in[0];
  const char* wqkv = (const char*)d_in[1];
  const char* wo = (const char*)d_in[2];
  const char* wg = (const char*)d_in[4];
  const char* wu = (const char*)d_in[5];
  const char* wd = (const char*)d_in[6];
  char* A2 = (char*)d_ws;
  char* B2 = A2 + A2_BYTES;
  phase1<<<256, 512, 0, stream>>>(x, wqkv, wo, wg, wu, wd, A2, B2);
  phase2<<<256, 256, 0, stream>>>(A2, B2, x, (char*)d_out);
}
```

```cpp
#include <hip/hip_runtime.h>
#include <hip/hip_bf16.h>
#include <stdint.h>

typedef __attribute__((ext_vector_type(4))) short s16x4;
typedef __attribute__((ext_vector_type(8))) short s16x8;
typedef __attribute__((ext_vector_type(2))) __bf16 bf2;
typedef __attribute__((ext_vector_type(8))) __bf16 bf8;
typedef __attribute__((ext_vector_type(4))) float f32x4;
typedef __attribute__((ext_vector_type(4))) unsigned u32x4;
typedef __attribute__((ext_vector_type(2))) unsigned u32x2;
#define LDS_AS __attribute__((address_space(3)))
#define DEVI __device__ __forceinline__

constexpr int S_ = 2048, D_ = 640, F_ = 1728;
constexpr int KS2 = 74;
constexpr size_t A2_BYTES = (size_t)32 * KS2 * 4096;
constexpr int STAT_OFF = 147456;
constexpr int LDS1_BYTES = STAT_OFF + 3328;
constexpr int QRS = 488;

#define STAMP(i)
#define STAMP2(i)
template <int ROLE> struct Cfg;
template <> struct Cfg<0> {
  static constexpr int WM = 2, WN = 4, MT = 4, NT = 6, WROW = 384, WSTAGE = 24576, XTILE = 16384,
                       XOFF = 98304, WI = 3, XE = 1, XO = 1;
};
template <> struct Cfg<1> {
  static constexpr int WM = 4, WN = 2, MT = 3, NT = 8, WROW = 512, WSTAGE = 16384, XTILE = 24576,
                       XOFF = 65536, WI = 2, XE = 2, XO = 1;
};
typedef __attribute__((ext_vector_type(2))) float f32x2;

DEVI void dma16(const char* sbase, unsigned voff, unsigned lds_off) {
  asm volatile("s_mov_b32 m0, %0\n\ts_nop 0\n\tglobal_load_lds_dwordx4 %1, %2" ::"s"(lds_off), "v"(voff), "s"(sbase) : "memory");
}
DEVI void dma16m(const char* sbase, unsigned voff, unsigned lds_off, unsigned long long mask) {
  asm volatile("s_mov_b32 m0, %0\n\ts_mov_b64 exec, %3\n\tglobal_load_lds_dwordx4 %1, %2\n\ts_mov_b64 exec, -1"
               ::"s"(lds_off), "v"(voff), "s"(sbase), "s"(mask) : "memory");
}
#define SB0 __builtin_amdgcn_sched_barrier(0)
template <int N> DEVI void wait_vm_lgkm0() {
  asm volatile("" ::: "memory");
  __builtin_amdgcn_s_waitcnt((N & 15) | ((N >> 4) << 14) | (7 << 4));
  asm volatile("" ::: "memory");
}
DEVI void lds_barrier() {
  asm volatile("" ::: "memory");
  __builtin_amdgcn_s_waitcnt(0xC07F);
  __builtin_amdgcn_s_barrier();
  asm volatile("" ::: "memory");
}
DEVI s16x4 tr4(unsigned a) { return __builtin_amdgcn_ds_read_tr16_b64_v4i16((LDS_AS s16x4*)(uintptr_t)a); }
DEVI bf8 cat8(s16x4 lo, s16x4 hi) { return __builtin_bit_cast(bf8, __builtin_shufflevector(lo, hi, 0, 1, 2, 3, 4, 5, 6, 7)); }
DEVI bf8 lds_b128(unsigned a) { return __builtin_bit_cast(bf8, *(LDS_AS u32x4*)(uintptr_t)a); }
DEVI u32x2 lds_b64(unsigned a) { return *(LDS_AS u32x2*)(uintptr_t)a; }
DEVI unsigned pk2(float a, float b) {
  bf2 v = {(__bf16)a, (__bf16)b};
  return __builtin_bit_cast(unsigned, v);
}
DEVI f32x4 mfma(bf8 a, bf8 b, f32x4 c) { return __builtin_amdgcn_mfma_f32_16x16x32_bf16(a, b, c, 0, 0, 0); }
DEVI float fbits(unsigned u) { return __builtin_bit_cast(float, u); }
DEVI unsigned ubits(float f) { return __builtin_bit_cast(unsigned, f); }
#define P2DEP 4
#define ST_AUX 16
typedef __amdgpu_buffer_rsrc_t rsrc_t;
DEVI rsrc_t mk_rsrc(void* p, unsigned bytes) { return __builtin_amdgcn_make_buffer_rsrc(p, 0, bytes, 0x00020000); }
DEVI void bst64(rsrc_t r, unsigned off, u32x2 v) { __builtin_amdgcn_raw_buffer_store_b64(v, r, off, 0, ST_AUX); }
DEVI void bst128(rsrc_t r, unsigned off, u32x4 v) { __builtin_amdgcn_raw_buffer_store_b128(v, r, off, 0, ST_AUX); }
constexpr unsigned OOB = 0x80000000u;
DEVI float vmax(float a, float b) { float r; asm("v_max_f32 %0, %1, %2" : "=v"(r) : "v"(a), "v"(b)); return r; }
DEVI float red_max(float v) {
  auto a = __builtin_amdgcn_permlane16_swap(ubits(v), ubits(v), false, false);
  v = vmax(fbits(a[0]), fbits(a[1]));
  auto b = __builtin_amdgcn_permlane32_swap(ubits(v), ubits(v), false, false);
  return vmax(fbits(b[0]), fbits(b[1]));
}
DEVI float red_sum(float v) {
  auto a = __builtin_amdgcn_permlane16_swap(ubits(v), ubits(v), false, false);
  v = fbits(a[0]) + fbits(a[1]);
  auto b = __builtin_amdgcn_permlane32_swap(ubits(v), ubits(v), false, false);
  return fbits(b[0]) + fbits(b[1]);
}
DEVI unsigned a2_off(int r, int k) {
  return (unsigned)(((r >> 6) * KS2 + (k >> 5)) * 4096 + ((r >> 4) & 3) * 1024 + ((((k >> 3) & 3) * 16 + (r & 15)) * 16) +
                    ((k >> 2) & 1) * 8);
}
template <int ROLE> constexpr int n_issue(int t) {
  return (t <= 15 ? Cfg<ROLE>::WI : 0) + (t <= 14 ? (((t + 5) & 1) ? Cfg<ROLE>::XO : Cfg<ROLE>::XE) : 0);
}

template <int ROLE>
DEVI void phase1_body(const char* __restrict__ x, const char* __restrict__ wa, const char* __restrict__ wb,
                      const char* __restrict__ wo, const char* __restrict__ wd, char* __restrict__ A2,
                      char* __restrict__ B2, int rb, int cg, unsigned lds0, int job) {
  using C = Cfg<ROLE>;
  constexpr int MT = C::MT, NT = C::NT;
  const int tid = threadIdx.x, lane = tid & 63;
  const int w = __builtin_amdgcn_readfirstlane(tid >> 6);
  const int g = lane >> 4, m = lane & 15, q = m >> 2, p = lane & 3;
  const int wm = w / C::WN, wn = w % C::WN;
  const int rbase = ROLE == 0 ? rb * 127 - 1 : rb * 176 - 16;
  STAMP(0);

  unsigned wof[C::WI];
  unsigned long long wmask[C::WI];
  constexpr int WSTRIDE = ROLE == 0 ? F_ * 2 : 3 * D_ * 2;
  const char* wbase;
  if (ROLE == 0) {
    wbase = (w < 4 ? wa : wb) + cg * 384;
#pragma unroll
    for (int i = 0; i < C::WI; ++i) {
      const int idx = ((w & 3) * 3 + i) * 64 + lane;
      const int row = idx / 24, pc = idx % 24;
      const int f = ((row >> 1) & 1) | ((row >> 2) & 2);
      wof[i] = row * WSTRIDE + (((pc >> 1) ^ f) * 32) + (pc & 1) * 16;
    }
  } else {
    wbase = wa + cg * 160;
#pragma unroll
    for (int i = 0; i < C::WI; ++i) {
      const int row = 4 * w + 2 * i + (lane >> 5), pc = lane & 31;
      const int f = (row & 3) | ((row >> 1) & 4);
      const int c = 2 * ((pc >> 1) ^ f) + (pc & 1);
      const int part = c / 10, cc = c % 10;
      const int poff = part == 0 ? 1280 : (part == 1 ? 2560 : 0);
      wof[i] = row * WSTRIDE + poff + cc * 16;
      wmask[i] = __ballot(c < 30);
    }
  }
  constexpr int XI = C::XE + C::XO;
  unsigned xof[XI];
#pragma unroll
  for (int i = 0; i < XI; ++i) {
    const int lr = (i * 8 + w) * 8 + (lane >> 3);
    int gr = rbase + lr;
    gr = gr < 0 ? 0 : (gr > S_ - 1 ? S_ - 1 : gr);
    xof[i] = gr * 1280 + ((lane & 7) ^ ((lr >> 1) & 7)) * 16;
  }
  const unsigned long long xmask0 = w == 0 ? ~0xffull : ~0ull;
  const unsigned wdst = lds0 + w * (C::WI * 1024);
  const unsigned xdst = lds0 + C::XOFF + w * 1024;
  auto dma_w = [&](int d, int stage) {
    const char* b = wbase + (size_t)stage * (32 * WSTRIDE);
    if (ROLE == 0) dma16(b, wof[d], wdst + (stage & 3) * C::WSTAGE + d * 1024);
    else dma16m(b, wof[d], wdst + (stage & 3) * C::WSTAGE + d * 1024, wmask[d]);
  };
  auto dma_x = [&](int i, int tile, int buf) {
    if (i == 0) dma16m(x + tile * 128, xof[i], xdst + buf * C::XTILE, xmask0);
    else dma16(x + tile * 128, xof[i], xdst + buf * C::XTILE + i * 8192);
  };
  auto issue_w = [&](int stage) {
#pragma unroll
    for (int i = 0; i < C::WI; ++i) dma_w(i, stage);
  };
  auto issue_x = [&](int odd, int tile) {
#pragma unroll
    for (int i = (odd ? C::XE : 0); i < (odd ? XI : C::XE); ++i) dma_x(i, tile, tile);
  };
  issue_x(0, 0);
  issue_w(0); issue_x(1, 0);
  issue_w(1); issue_x(0, 1);
  const u32x4 ones4 = {0x3F803F80u, 0x3F803F80u, 0x3F803F80u, 0x3F803F80u};
  if (tid < 24) *(LDS_AS u32x4*)(uintptr_t)(lds0 + C::XOFF + (tid >> 3) * C::XTILE + (tid & 7) * 16) = ones4;
  if (ROLE == 1 && tid >= 256) {
    const int t2 = tid - 256, row = (t2 >> 1) & 31;
    const int f = (row & 3) | ((row >> 1) & 4);
    *(LDS_AS u32x4*)(uintptr_t)(lds0 + (t2 >> 6) * C::WSTAGE + row * 512 + (15 ^ f) * 32 + (t2 & 1) * 16) = ones4;
  }

  constexpr int NWA = ROLE == 0 ? 1 : 4;
  unsigned wad[NWA], xad[MT];
  if (ROLE == 0) {
    const int f = ((q >> 1) & 1) | ((g & 1) << 1);
    wad[0] = lds0 + (8 * g + q) * 384 + ((wn ^ f) * 32) + p * 8;
  } else {
    const int f = q | ((g & 1) << 2);
#pragma unroll
    for (int e = 0; e < NWA; ++e) wad[e] = lds0 + (8 * g + q) * 512 + (((wn * 4 + e) ^ f) * 32) + p * 8;
  }
  int lrow[MT];
#pragma unroll
  for (int j = 0; j < MT; ++j) {
    const int act = ROLE == 0 ? ((j + wn) & 3) : (wn ? 2 - j : j);
    lrow[j] = (wm * MT + act) * 16 + m;
    xad[j] = lds0 + C::XOFF + lrow[j] * 128 + ((g ^ ((lrow[j] >> 1) & 7)) * 16);
  }
  f32x4 acc[MT][NT];
#pragma unroll
  for (int j = 0; j < MT; ++j)
#pragma unroll
    for (int n = 0; n < NT; ++n) acc[j][n] = (f32x4){0.f, 0.f, 0.f, 0.f};
  f32x4 st0 = {0.f, 0.f, 0.f, 0.f}, st1 = {0.f, 0.f, 0.f, 0.f};
  const bf8 onesf = __builtin_bit_cast(bf8, ones4);
  bf8 xf[2][MT];
  s16x4 wh[2][NT][2];

  auto read_one = [&](int par, int r, unsigned woff, unsigned xoff) {
    if (r < MT) {
      xf[par][r] = lds_b128((xad[r] + xoff) ^ (par ? 64u : 0u));
    } else {
      const int n = (r - MT) >> 1, hl = (r - MT) & 1;
      if (ROLE == 0) wh[par][n][hl] = tr4(wad[0] + woff + (n & 1) * 12288 + (n >> 1) * 128 + hl * 4 * 384);
      else wh[par][n][hl] = tr4(wad[n & 3] + woff + (n >> 2) * 256 + hl * 4 * 512);
    }
  };
  constexpr int NR = MT + 2 * NT;

  wait_vm_lgkm0<n_issue<ROLE>(-3)>();
  __builtin_amdgcn_s_barrier();
#pragma unroll
  for (int r = 0; r < NR; ++r) read_one(0, r, 0, 0);
  STAMP(1);

  int xr = 0;
#define JOB_HOOK
#define ITER(T, PAR, VM, DOW, DOX, DORD, DBL)                                                    \
  {                                                                                          \
    if (DORD) { wait_vm_lgkm0<VM>(); __builtin_amdgcn_s_barrier(); }                         \
    SB0;                                                                                     \
    const int xn = xr == 2 ? 0 : xr + 1;                                                     \
    const int xq = xn == 2 ? 0 : xn + 1;                                                     \
    const unsigned woff = (((T) + 1) & 3) * C::WSTAGE, xoff = (PAR ? xn : xr) * C::XTILE;    \
    _Pragma("unroll") for (int i = 0; i < 24; ++i) {                                         \
      const int n = i / MT, j = i % MT;                                                      \
      acc[j][n] = mfma(cat8(wh[PAR][n][0], wh[PAR][n][1]), xf[PAR][j], acc[j][n]);           \
      if (DORD && i < 20 && i * NR / 20 != (i + 1) * NR / 20) read_one(PAR ^ 1, i * NR / 20, woff, xoff); \
      if (DBL && i % 3 == 2) {     \
        const int V = 2 * (PAR) - 2 + (i / 12), d = (i / 3) & 3, pc_ = V + 5;                \
        if (d < C::WI) dma_w(d, V + 4);                                                      \
        else {                                                                               \
          const int xi = ((pc_ & 1) ? C::XE : 0) + (d - C::WI);                              \
          if (xi < ((pc_ & 1) ? XI : C::XE)) dma_x(xi, pc_ >> 1, (pc_ >> 1) % 3);            \
        }                                                                                    \
      }                                                                                      \
      if (!DBL && DOW && i % 6 == 2) {                                                       \
        const int d = i / 6;                                                                 \
        if (d < C::WI) dma_w(d, (T) + 4);                                                    \
        else if (DOX) {                                                                      \
          const int xi = (PAR ? 0 : C::XE) + (d - C::WI);                                    \
          if (xi < (PAR ? C::XE : XI)) dma_x(xi, ((T) + 5) >> 1, PAR ? xr : xq);             \
        }                                                                                    \
      }                                                                                      \
      JOB_HOOK                                                                               \
      if (i == 21) st0 = mfma(xf[PAR][0], xf[PAR][0], st0);                                  \
      if (i == 22) {                                                                         \
        if (ROLE == 0) st1 = mfma(onesf, xf[PAR][0], st1);                                   \
        else if (wn == PAR) st1 = mfma(xf[PAR][1], xf[PAR][1], st1);                         \
      }                                                                                      \
      SB0;                                                                                   \
    }                                                                                        \
    if (PAR) xr = xn;                                                                        \
  }
  constexpr int VMF = n_issue<ROLE>(0) + n_issue<ROLE>(1);
  ITER(0, 0, 0, true, true, true, true)
  ITER(1, 1, n_issue<ROLE>(-1), true, true, true, true)
  for (int t = 2; t < 14; t += 2) {
    ITER(t, 0, VMF, true, true, true, false)
    ITER(t + 1, 1, VMF, true, true, true, false)
  }
  ITER(14, 0, VMF, true, true, true, false)
  ITER(15, 1, VMF, true, false, true, false)
  ITER(16, 0, n_issue<ROLE>(14) + n_issue<ROLE>(15), false, false, true, false)
  ITER(17, 1, n_issue<ROLE>(15), false, false, true, false)
  unsigned jl[8];
  const int J = job + w < 1480 ? job + w : job + w - 1480;
  const int jks = J / 20, jcb = J % 20;
  const char* jsrc = (jks < 20 ? wo + (size_t)(jks * 32) * 1280 : wd + (size_t)(jks * 32 - 640) * 1280) + jcb * 64;
#undef JOB_HOOK
#define JOB_HOOK if (i < 8) jl[i] = *(const unsigned*)(jsrc + (unsigned)((8 * g + i) * 1280 + m * 4));
  ITER(18, 0, 0, false, false, true, false)
#undef JOB_HOOK
#define JOB_HOOK
  ITER(19, 1, 0, false, false, false, false)
#undef ITER
#undef JOB_HOOK
  STAMP(2);

  LDS_AS float* rsum = (LDS_AS float*)(uintptr_t)(lds0 + STAT_OFF);
  LDS_AS float* rsq = rsum + 192;
  LDS_AS float* csum = rsum + 448;
  {
    const bool diag = g == q;
    const unsigned e0 = p == 0 ? ~0u : 0u, e1 = p == 1 ? ~0u : 0u, e2 = p == 2 ? ~0u : 0u, e3 = p == 3 ? ~0u : 0u;
    const float d0 = fbits((ubits(st0[0]) & e0) | (ubits(st0[1]) & e1) | (ubits(st0[2]) & e2) | (ubits(st0[3]) & e3));
    if (ROLE == 0) {
      if (diag) { rsq[lrow[0]] = d0; rsum[lrow[0]] = st1[0]; }
    } else {
      const float d1 = fbits((ubits(st1[0]) & e0) | (ubits(st1[1]) & e1) | (ubits(st1[2]) & e2) | (ubits(st1[3]) & e3));
      if (diag) {
        rsq[lrow[0]] = d0;
        rsq[wn ? 192 + wm * 16 + m : lrow[1]] = d1;
        if (wn) {
#pragma unroll
          for (int j = 0; j < MT; ++j) rsum[lrow[j]] = acc[j][7][0];
        }
      }
    }
  }
  if (wm == 0) {
#pragma unroll
    for (int j = 0; j < MT; ++j) {
      if (lrow[j] == 0) {
#pragma unroll
        for (int n = 0; n < NT; ++n) *(LDS_AS f32x4*)(csum + (wn * NT + n) * 16 + 4 * g) = acc[j][n];
      }
    }
  }
  lds_barrier();
  float ra[MT], rbv[MT], ir2[MT], k1[MT], k3[MT];
#pragma unroll
  for (int j = 0; j < MT; ++j) {
    const float mean = rsum[lrow[j]] * (1.f / D_);
    float sq = rsq[lrow[j]];
    if (ROLE == 1 && j == 1) sq += rsq[192 + wm * 16 + m];
    const float var = fmaxf(sq * (1.f / D_) - mean * mean, 0.f);
    ra[j] = __builtin_amdgcn_rsqf(var + 1e-5f);
    rbv[j] = ROLE == 0 ? -mean : -mean * ra[j];
    ir2[j] = var + 1e-5f;
    k1[j] = -2.3022082f * ra[j];
    k3[j] = -0.10294324f * ra[j] * ra[j] * ra[j];
  }
  STAMP(3);
  {
    const int n0 = jcb * 32 + 2 * m, nt = n0 >> 4;
    const rsrc_t b2r = mk_rsrc(B2, 8 * KS2 * 5120);
    const unsigned dst = job + w < 1480 ? (unsigned)((((nt / 5) * KS2 + jks) * 5 + nt % 5) * 1024 + (g * 16 + (n0 & 15)) * 16) : OOB;
#pragma unroll
    for (int i = 0; i < 2; ++i) {
      u32x4 o;
#pragma unroll
      for (int j = 0; j < 4; ++j) o[j] = __builtin_amdgcn_perm(jl[2 * j + 1], jl[2 * j], i ? 0x07060302u : 0x05040100u);
      bst128(b2r, dst + i * 16, o);
    }
  }
  const rsrc_t a2r = mk_rsrc(A2, (unsigned)A2_BYTES);
  if (ROLE == 0) {
    unsigned so[2];
#pragma unroll
    for (int jp = 0; jp < 2; ++jp) {
      const int lr = (wm * 4 + ((2 * jp + (g & 1) + wn) & 3)) * 16 + m;
      const int gr = rbase + lr;
      so[jp] = (lr >= 1 && gr < S_) ? a2_off(gr, 640 + cg * 192 + wn * 16 + (g >> 1) * 8) : OOB;
    }
#pragma unroll
    for (int a = 0; a < 3; ++a) {
      const f32x4 cg4 = *(LDS_AS f32x4*)(csum + (wn * NT + 2 * a) * 16 + 4 * g);
      const f32x4 cu4 = *(LDS_AS f32x4*)(csum + (wn * NT + 2 * a + 1) * 16 + 4 * g);
#pragma unroll
      for (int jp = 0; jp < 2; ++jp) {
        unsigned hp[2][2];
#pragma unroll
        for (int jj = 0; jj < 2; ++jj) {
          const int j = 2 * jp + jj;
#pragma unroll
          for (int r = 0; r < 2; ++r) {
            const f32x2 ag = {acc[j][2 * a][2 * r], acc[j][2 * a][2 * r + 1]};
            const f32x2 au = {acc[j][2 * a + 1][2 * r], acc[j][2 * a + 1][2 * r + 1]};
            const f32x2 cgv = {cg4[2 * r], cg4[2 * r + 1]}, cuv = {cu4[2 * r], cu4[2 * r + 1]};
            const f32x2 tg = cgv * rbv[j] + ag;
            const f32x2 tu = cuv * rbv[j] + au;
            const f32x2 z = tg * (tg * tg * k3[j] + k1[j]);
            const f32x2 gu = tg * tu;
            const f32x2 ev = {__builtin_amdgcn_exp2f(z[0]), __builtin_amdgcn_exp2f(z[1])};
            const f32x2 dn = ev * ir2[j] + ir2[j];
            const float h0 = gu[0] * __builtin_amdgcn_rcpf(dn[0]);
            const float h1 = gu[1] * __builtin_amdgcn_rcpf(dn[1]);
            hp[jj][r] = pk2(h0, h1);
          }
        }
        auto s0 = __builtin_amdgcn_permlane16_swap(hp[0][0], hp[1][0], false, false);
        auto s1 = __builtin_amdgcn_permlane16_swap(hp[0][1], hp[1][1], false, false);
        bst128(a2r, so[jp] + a * 8192, (u32x4){s0[0], s1[0], s0[1], s1[1]});
      }
    }
  } else {
#pragma unroll
    for (int n = 0; n < NT; ++n) {
      const int tau = (n >> 2) * 8 + wn * 4 + (n & 3);
      const f32x4 c4 = *(LDS_AS f32x4*)(csum + (wn * NT + n) * 16 + 4 * g);
      if (tau < 15) {
#pragma unroll
        for (int j = 0; j < MT; ++j) {
          const f32x4 v = acc[j][n] * ra[j] + c4 * rbv[j];
          *(LDS_AS u32x2*)(uintptr_t)(lds0 + lrow[j] * QRS + tau * 32 + g * 8) = (u32x2){pk2(v[0], v[1]), pk2(v[2], v[3])};
        }
      }
    }
    lds_barrier();
    STAMP(4);
    const float sc = 0.22360679775f * 1.44269504089f;
    constexpr int NI = 6;
    const int h = w & 3, mt0 = 1 + (w >> 2);
    const bool odd = m & 1;
    const bool lea = 4 * g + (m & 1) <= m, leb = 4 * g + 2 + (m & 1) <= m;
    const unsigned mpar = odd ? 0xffff0000u : 0x0000ffffu;
    unsigned m0a = lea ? 0u : mpar, m1a = lea ? mpar : 0u, m0b = leb ? 0u : mpar, m1b = leb ? mpar : 0u;
    unsigned qma = g == 3 ? 0u : ~0u, qmb = g >= 2 ? 0u : ~0u;
    asm volatile("" : "+v"(m0a), "+v"(m1a), "+v"(m0b), "+v"(m1b), "+v"(qma), "+v"(qmb));
    const unsigned rq = lds0 + ((mt0 - 1) * 16 + m) * QRS + h * 40 + g * 16;
    const unsigned va = lds0 + ((mt0 - 1) * 16 + 4 * g + q) * QRS + 160 + h * 40 + p * 8;
    unsigned rq2 = rq + 96 * QRS, va2 = va + 96 * QRS;
    asm volatile("" : "+v"(rq2), "+v"(va2));
    const int kq = cg * 80 + h * 20;
    const unsigned vo0 = a2_off(m, kq + 4 * g), vo1 = g == 0 ? a2_off(m, kq + 16) : OOB;
    bf8 v0[NI], v1[NI];
    f32x4 s0[NI], s1[NI];
    const f32x4 z4 = {0.f, 0.f, 0.f, 0.f};
#pragma unroll
    for (int ii = 0; ii < NI; ++ii) {
      const unsigned r0 = (ii < 3 ? rq : rq2) + (ii % 3) * 32 * QRS, vv = (ii < 3 ? va : va2) + (ii % 3) * 32 * QRS;
      const u32x2 k0a = lds_b64(r0), k0b = lds_b64(r0 + 8);
      const u32x2 k1a = lds_b64(r0 + 16 * QRS), k1b = lds_b64(r0 + 16 * QRS + 8);
      const u32x2 qa = lds_b64(r0 + 16 * QRS + 320), qb = lds_b64(r0 + 16 * QRS + 328);
      v0[ii] = cat8(tr4(vv), tr4(vv + 16 * QRS));
      v1[ii] = cat8(tr4(vv + 32), tr4(vv + 16 * QRS + 32));
      const bf8 qf = __builtin_bit_cast(bf8, (u32x4){qa[0] & qma, qa[1] & qma, qb[0] & qmb, qb[1] & qmb});
      s0[ii] = mfma(__builtin_bit_cast(bf8, (u32x4){k0a[0], k0a[1], k0b[0], k0b[1]}), qf, z4);
      s1[ii] = mfma(__builtin_bit_cast(bf8, (u32x4){k1a[0], k1a[1], k1b[0], k1b[1]}), qf, z4);
    }
    u32x2 oa[NI], ob[NI];
    const unsigned ev = odd ? 0u : ~0u, od = ~ev, sa = lea ? ~0u : 0u, sb = leb ? ~0u : 0u;
    const bool bad = rbase + (mt0 - 1) * 16 < 0;
    const unsigned bada = (bad && !lea) ? ~0u : 0u, badb = (bad && !leb) ? ~0u : 0u;
    unsigned k00 = ev & ~sa, k01 = od & ~sa, k10 = ev & sa, k11 = od & sa;
    unsigned k02 = ev & ~sb, k03 = od & ~sb, k12 = ev & sb, k13 = od & sb;
    asm volatile("" : "+v"(k00), "+v"(k01), "+v"(k10), "+v"(k11), "+v"(k02), "+v"(k03), "+v"(k12), "+v"(k13));
    float la[NI], lb[NI], mx[NI];
#pragma unroll
    for (int ii = 0; ii < NI; ++ii) {
      unsigned ua = (ubits(s0[ii][0]) & k00) | (ubits(s0[ii][1]) & k01) | (ubits(s1[ii][0]) & k10) | (ubits(s1[ii][1]) & k11);
      unsigned ub = (ubits(s0[ii][2]) & k02) | (ubits(s0[ii][3]) & k03) | (ubits(s1[ii][2]) & k12) | (ubits(s1[ii][3]) & k13);
      if (ii == 0) {
        ua = (ua & ~bada) | (0xff7fffffu & bada);
        ub = (ub & ~badb) | (0xff7fffffu & badb);
      }
      la[ii] = fbits(ua); lb[ii] = fbits(ub);
      mx[ii] = vmax(la[ii], lb[ii]);
    }
#pragma unroll
    for (int ii = 0; ii < NI; ++ii) mx[ii] = red_max(mx[ii]) * sc;
#pragma unroll
    for (int ii = 0; ii < NI; ++ii) {
      la[ii] = __builtin_amdgcn_exp2f(la[ii] * sc - mx[ii]);
      lb[ii] = __builtin_amdgcn_exp2f(lb[ii] * sc - mx[ii]);
      mx[ii] = la[ii] + lb[ii];
    }
#pragma unroll
    for (int ii = 0; ii < NI; ++ii) mx[ii] = __builtin_amdgcn_rcpf(red_sum(mx[ii]));
#pragma unroll
    for (int ii = 0; ii < NI; ++ii) {
      const float pa_ = la[ii] * mx[ii], pb_ = lb[ii] * mx[ii];
      const unsigned pa = pk2(pa_, pa_), pb = pk2(pb_, pb_);
      const bf8 pf = __builtin_bit_cast(bf8, (u32x4){pa & m0a, pb & m0b, pa & m1a, pb & m1b});
      s0[ii] = mfma(v0[ii], pf, z4);
      s1[ii] = mfma(v1[ii], pf, z4);
    }
#pragma unroll
    for (int ii = 0; ii < NI; ++ii) {
      oa[ii] = (u32x2){pk2(s0[ii][0], s0[ii][1]), pk2(s0[ii][2], s0[ii][3])};
      ob[ii] = (u32x2){pk2(s1[ii][0], s1[ii][1]), pk2(s1[ii][2], s1[ii][3])};
    }
#pragma unroll
    for (int ii = 0; ii < NI; ++ii) {
      const int mt = mt0 + 2 * ii, T = rb * 11 - 1 + mt;
      if (mt < 12 && T < 128) {
        const int so = (T >> 2) * (KS2 * 4096) + (T & 3) * 1024;
        __builtin_amdgcn_raw_buffer_store_b64(oa[ii], a2r, vo0, so, ST_AUX);
        __builtin_amdgcn_raw_buffer_store_b64(ob[ii], a2r, vo1, so, ST_AUX);
      }
    }
  }
  STAMP(5);
}

__global__ __launch_bounds__(512, 2) void phase1(const char* __restrict__ x, const char* __restrict__ wqkv,
                                                 const char* __restrict__ wo, const char* __restrict__ wg,
                                                 const char* __restrict__ wu, const char* __restrict__ wd,
                                                 char* __restrict__ A2, char* __restrict__ B2) {
  __shared__ __attribute__((aligned(1024))) char smem[LDS1_BYTES];
  const unsigned lds0 = (unsigned)(uintptr_t)(LDS_AS char*)smem;
  const int b = blockIdx.x, xcd = b & 7, slot = b >> 3;
  if (false) {
    unsigned long long pc0;
    int dlen;
    asm volatile("s_getpc_b64 %0\nP1_REF_LBL:\n\ts_mov_b32 %1, P2_TAIL_LBL-P1_REF_LBL" : "=s"(pc0), "=s"(dlen));
    const unsigned long long lo = pc0 & ~15ull;
    const unsigned len = dlen > 0 ? (unsigned)dlen : 0u;
    unsigned o = ((slot - 12) * 512 + threadIdx.x) * 16;
    unsigned sink = 0;
    for (; o + 16 <= len && o < 131072u; o += 32768) sink ^= (*(const u32x4*)((const char*)lo + o))[0];
    asm volatile("" ::"v"(sink));
  }
  if (slot < 12) {
    const int hg = 2 * (xcd & 3) + slot / 6, rb = 6 * (xcd >> 2) + slot % 6;
    phase1_body<1>(x, wqkv, wqkv, wo, wd, A2, B2, rb, hg, lds0, (xcd * 12 + slot) * 8);
  } else {
    const int L = xcd * 20 + (slot - 12);
    if (L >= 153) return;
    int rb, cg;
    if (L < 72) { cg = L >> 3; rb = L & 7; } else { cg = (L - 72) / 9; rb = 8 + (L - 72) % 9; }
    phase1_body<0>(x, wg, wu, wo, wd, A2, B2, rb, cg, lds0, (96 + L) * 8);
  }
}

__global__ __launch_bounds__(256, 1) void phase2(const char* __restrict__ A2, const char* __restrict__ B2,
                                                 const char* __restrict__ x, char* __restrict__ out) {
  __shared__ __attribute__((aligned(16))) f32x4 red[4][3][5][64];
  const int tid = threadIdx.x, lane = tid & 63;
  const int w = __builtin_amdgcn_readfirstlane(tid >> 6);
  const int g = lane >> 4, m = lane & 15;
  const int b = blockIdx.x, xcd = b & 7, slot = b >> 3;
  const int rb = (xcd >> 1) * 8 + (slot & 7), cg = (xcd & 1) * 4 + (slot >> 3);
  const int pw = ((w & 1) << 1) | (w >> 1);
  const char* ap = A2 + (size_t)(rb * KS2) * 4096;
  const char* bp = B2 + (size_t)(cg * KS2) * 5120;
  const unsigned lo = lane * 16;
  unsigned la[4];
#pragma unroll
  for (int j = 0; j < 4; ++j) la[j] = lo + (j ^ pw) * 1024;
  STAMP2(0);
  constexpr int DEP = P2DEP, NI = 19;
  u32x4 af[DEP][4], bfr[DEP][5];
  f32x4 acc[4][5];
#pragma unroll
  for (int i = 0; i < 4; ++i)
#pragma unroll
    for (int n = 0; n < 5; ++n) acc[i][n] = (f32x4){0.f, 0.f, 0.f, 0.f};
  auto load1 = [&](int i, int u) {
    const int ks = i == NI - 1 ? 72 + (w >> 1) : w + 4 * i;
    if (u < 5) bfr[i % DEP][u] = *(const u32x4*)(bp + ks * 5120 + (lo + u * 1024));
    else if (i < NI - 1 || u < 7) af[i % DEP][u - 5] = *(const u32x4*)(ap + ks * 4096 + la[u - 5]);
  };
  const int orow = rb * 64 + pw * 16 + m;
  const size_t ooff = (size_t)orow * 1280 + (cg * 80 + 4 * g) * 2;
  u32x2 xr[5];
#pragma unroll
  for (int i = 0; i < DEP - 1; ++i)
#pragma unroll
    for (int u = 0; u < 9; ++u) load1(i, u);
#pragma unroll
  for (int n = 0; n < 5; ++n) xr[n] = *(const u32x2*)(x + ooff + n * 32);
  SB0;
#pragma unroll
  for (int i = 0; i < NI; ++i) {
#pragma unroll
    for (int u = 0; u < (i == NI - 1 ? 10 : 20); ++u) {
      const int j = u / 5, n = u % 5;
      acc[j][n] = mfma(__builtin_bit_cast(bf8, bfr[i % DEP][n]), __builtin_bit_cast(bf8, af[i % DEP][j]), acc[j][n]);
      if ((u & 1) == 0 && u < 18 && i + DEP - 1 < NI) load1(i + DEP - 1, u >> 1);
      SB0;
    }
    if (i == NI - 2) {
#pragma unroll
      for (int j = 2; j < 4; ++j)
#pragma unroll
        for (int n = 0; n < 5; ++n) red[j ^ pw][j - 1][n][lane] = acc[j][n];
    }
  }
  STAMP2(1);
#pragma unroll
  for (int n = 0; n < 5; ++n) red[1 ^ pw][0][n][lane] = acc[1][n];
  __syncthreads();
#pragma unroll
  for (int n = 0; n < 5; ++n) {
    const f32x4 s = (acc[0][n] + red[pw][0][n][lane]) + (red[pw][1][n][lane] + red[pw][2][n][lane]);
    const float x0 = fbits(xr[n][0] << 16), x1 = fbits(xr[n][0] & 0xffff0000u);
    const float x2 = fbits(xr[n][1] << 16), x3 = fbits(xr[n][1] & 0xffff0000u);
    *(u32x2*)(out + ooff + n * 32) = (u32x2){pk2(s[0] + x0, s[1] + x1), pk2(s[2] + x2, s[3] + x3)};
  }

  STAMP2(2);
}

extern "C" void kernel_launch(void* const* d_in, const int* in_sizes, int n_in, void* d_out, int out_size, void* d_ws,
                              size_t ws_size, hipStream_t stream) {
  const char* x = (const char*)d_in[0];
  const char* wqkv = (const char*)d_in[1];
  const char* wo = (const char*)d_in[2];
  const char* wg = (const char*)d_in[4];
  const char* wu = (const char*)d_in[5];
  const char* wd = (const char*)d_in[6];
  char* A2 = (char*)d_ws;
  char* B2 = A2 + A2_BYTES;
  phase1<<<256, 512, 0, stream>>>(x, wqkv, wo, wg, wu, wd, A2, B2);
  phase2<<<256, 256, 0, stream>>>(A2, B2, x, (char*)d_out);
}
```

```cpp
#include <hip/hip_runtime.h>
#include <hip/hip_bf16.h>
#include <stdint.h>

typedef __attribute__((ext_vector_type(4))) short s16x4;
typedef __attribute__((ext_vector_type(8))) short s16x8;
typedef __attribute__((ext_vector_type(2))) __bf16 bf2;
typedef __attribute__((ext_vector_type(8))) __bf16 bf8;
typedef __attribute__((ext_vector_type(4))) float f32x4;
typedef __attribute__((ext_vector_type(4))) unsigned u32x4;
typedef __attribute__((ext_vector_type(2))) unsigned u32x2;
#define LDS_AS __attribute__((address_space(3)))
#define DEVI __device__ __forceinline__

constexpr int S_ = 2048, D_ = 640, F_ = 1728;
constexpr int KS2 = 74;
constexpr size_t A2_BYTES = (size_t)32 * KS2 * 4096;
constexpr int STAT_OFF = 147456;
constexpr int LDS1_BYTES = STAT_OFF + 3328;
constexpr int QRS = 488;

#define STAMP(i)
#define STAMP2(i)
template <int ROLE> struct Cfg;
template <> struct Cfg<0> {
  static constexpr int WM = 2, WN = 4, MT = 4, NT = 6, WROW = 384, WSTAGE = 24576, XTILE = 16384,
                       XOFF = 98304, WI = 3, XE = 1, XO = 1;
};
template <> struct Cfg<1> {
  static constexpr int WM = 4, WN = 2, MT = 3, NT = 8, WROW = 512, WSTAGE = 16384, XTILE = 24576,
                       XOFF = 65536, WI = 2, XE = 2, XO = 1;
};
typedef __attribute__((ext_vector_type(2))) float f32x2;

DEVI void dma16(const char* sbase, unsigned voff, unsigned lds_off) {
  asm volatile("s_mov_b32 m0, %0\n\ts_nop 0\n\tglobal_load_lds_dwordx4 %1, %2" ::"s"(lds_off), "v"(voff), "s"(sbase) : "memory");
}
DEVI void dma16m(const char* sbase, unsigned voff, unsigned lds_off, unsigned long long mask) {
  asm volatile("s_mov_b32 m0, %0\n\ts_mov_b64 exec, %3\n\tglobal_load_lds_dwordx4 %1, %2\n\ts_mov_b64 exec, -1"
               ::"s"(lds_off), "v"(voff), "s"(sbase), "s"(mask) : "memory");
}
#define SB0 __builtin_amdgcn_sched_barrier(0)
template <int N> DEVI void wait_vm_lgkm0() {
  asm volatile("" ::: "memory");
  __builtin_amdgcn_s_waitcnt((N & 15) | ((N >> 4) << 14) | (7 << 4));
  asm volatile("" ::: "memory");
}
DEVI void lds_barrier() {
  asm volatile("" ::: "memory");
  __builtin_amdgcn_s_waitcnt(0xC07F);
  __builtin_amdgcn_s_barrier();
  asm volatile("" ::: "memory");
}
DEVI s16x4 tr4(unsigned a) { return __builtin_amdgcn_ds_read_tr16_b64_v4i16((LDS_AS s16x4*)(uintptr_t)a); }
DEVI bf8 cat8(s16x4 lo, s16x4 hi) { return __builtin_bit_cast(bf8, __builtin_shufflevector(lo, hi, 0, 1, 2, 3, 4, 5, 6, 7)); }
DEVI bf8 lds_b128(unsigned a) { return __builtin_bit_cast(bf8, *(LDS_AS u32x4*)(uintptr_t)a); }
DEVI u32x2 lds_b64(unsigned a) { return *(LDS_AS u32x2*)(uintptr_t)a; }
DEVI unsigned pk2(float a, float b) {
  bf2 v = {(__bf16)a, (__bf16)b};
  return __builtin_bit_cast(unsigned, v);
}
DEVI f32x4 mfma(bf8 a, bf8 b, f32x4 c) { return __builtin_amdgcn_mfma_f32_16x16x32_bf16(a, b, c, 0, 0, 0); }
DEVI float fbits(unsigned u) { return __builtin_bit_cast(float, u); }
DEVI unsigned ubits(float f) { return __builtin_bit_cast(unsigned, f); }
#define P2DEP 4
#define ST_AUX 16
typedef __amdgpu_buffer_rsrc_t rsrc_t;
DEVI rsrc_t mk_rsrc(void* p, unsigned bytes) { return __builtin_amdgcn_make_buffer_rsrc(p, 0, bytes, 0x00020000); }
DEVI void bst64(rsrc_t r, unsigned off, u32x2 v) { __builtin_amdgcn_raw_buffer_store_b64(v, r, off, 0, ST_AUX); }
DEVI void bst128(rsrc_t r, unsigned off, u32x4 v) { __builtin_amdgcn_raw_buffer_store_b128(v, r, off, 0, ST_AUX); }
constexpr unsigned OOB = 0x80000000u;
DEVI float vmax(float a, float b) { float r; asm("v_max_f32 %0, %1, %2" : "=v"(r) : "v"(a), "v"(b)); return r; }
DEVI float red_max(float v) {
  auto a = __builtin_amdgcn_permlane16_swap(ubits(v), ubits(v), false, false);
  v = vmax(fbits(a[0]), fbits(a[1]));
  auto b = __builtin_amdgcn_permlane32_swap(ubits(v), ubits(v), false, false);
  return vmax(fbits(b[0]), fbits(b[1]));
}
DEVI float red_sum(float v) {
  auto a = __builtin_amdgcn_permlane16_swap(ubits(v), ubits(v), false, false);
  v = fbits(a[0]) + fbits(a[1]);
  auto b = __builtin_amdgcn_permlane32_swap(ubits(v), ubits(v), false, false);
  return fbits(b[0]) + fbits(b[1]);
}
DEVI unsigned a2_off(int r, int k) {
  return (unsigned)(((r >> 6) * KS2 + (k >> 5)) * 4096 + ((r >> 4) & 3) * 1024 + ((((k >> 3) & 3) * 16 + (r & 15)) * 16) +
                    ((k >> 2) & 1) * 8);
}
template <int ROLE> constexpr int n_issue(int t) {
  return (t <= 15 ? Cfg<ROLE>::WI : 0) + (t <= 14 ? (((t + 5) & 1) ? Cfg<ROLE>::XO : Cfg<ROLE>::XE) : 0);
}

DEVI const char* job_src(const char* wo, const char* wd, int J) {
  const int jks = J / 20, jcb = J % 20;
  return (jks < 20 ? wo + (size_t)(jks * 32) * 1280 : wd + (size_t)(jks * 32 - 640) * 1280) + jcb * 64;
}
DEVI void job_store(char* B2, int J, bool ok, const unsigned* jl, int g, int m) {
  const int jks = J / 20, jcb = J % 20;
  const int n0 = jcb * 32 + 2 * m, nt = n0 >> 4;
  const rsrc_t b2r = mk_rsrc(B2, 8 * KS2 * 5120);
  const unsigned dst = ok ? (unsigned)((((nt / 5) * KS2 + jks) * 5 + nt % 5) * 1024 + (g * 16 + (n0 & 15)) * 16) : OOB;
#pragma unroll
  for (int i = 0; i < 2; ++i) {
    u32x4 o;
#pragma unroll
    for (int j = 0; j < 4; ++j) o[j] = __builtin_amdgcn_perm(jl[2 * j + 1], jl[2 * j], i ? 0x07060302u : 0x05040100u);
    bst128(b2r, dst + i * 16, o);
  }
}

template <int ROLE>
DEVI void phase1_body(const char* __restrict__ x, const char* __restrict__ wa, const char* __restrict__ wb,
                      const char* __restrict__ wo, const char* __restrict__ wd, char* __restrict__ A2,
                      char* __restrict__ B2, int rb, int cg, unsigned lds0, int job) {
  using C = Cfg<ROLE>;
  constexpr int MT = C::MT, NT = C::NT;
  const int tid = threadIdx.x, lane = tid & 63;
  const int w = __builtin_amdgcn_readfirstlane(tid >> 6);
  const int g = lane >> 4, m = lane & 15, q = m >> 2, p = lane & 3;
  const int wm = w / C::WN, wn = w % C::WN;
  const int rbase = ROLE == 0 ? rb * 127 - 1 : rb * 176 - 16;
  STAMP(0);

  unsigned wof[C::WI];
  unsigned long long wmask[C::WI];
  constexpr int WSTRIDE = ROLE == 0 ? F_ * 2 : 3 * D_ * 2;
  const char* wbase;
  if (ROLE == 0) {
    wbase = (w < 4 ? wa : wb) + cg * 384;
#pragma unroll
    for (int i = 0; i < C::WI; ++i) {
      const int idx = ((w & 3) * 3 + i) * 64 + lane;
      const int row = idx / 24, pc = idx % 24;
      const int f = ((row >> 1) & 1) | ((row >> 2) & 2);
      wof[i] = row * WSTRIDE + (((pc >> 1) ^ f) * 32) + (pc & 1) * 16;
    }
  } else {
    wbase = wa + cg * 160;
#pragma unroll
    for (int i = 0; i < C::WI; ++i) {
      const int row = 4 * w + 2 * i + (lane >> 5), pc = lane & 31;
      const int f = (row & 3) | ((row >> 1) & 4);
      const int c = 2 * ((pc >> 1) ^ f) + (pc & 1);
      const int part = c / 10, cc = c % 10;
      const int poff = part == 0 ? 1280 : (part == 1 ? 2560 : 0);
      wof[i] = row * WSTRIDE + poff + cc * 16;
      wmask[i] = __ballot(c < 30);
    }
  }
  constexpr int XI = C::XE + C::XO;
  unsigned xof[XI];
#pragma unroll
  for (int i = 0; i < XI; ++i) {
    const int lr = (i * 8 + w) * 8 + (lane >> 3);
    int gr = rbase + lr;
    gr = gr < 0 ? 0 : (gr > S_ - 1 ? S_ - 1 : gr);
    xof[i] = gr * 1280 + ((lane & 7) ^ ((lr >> 1) & 7)) * 16;
  }
  const unsigned long long xmask0 = w == 0 ? ~0xffull : ~0ull;
  const unsigned wdst = lds0 + w * (C::WI * 1024);
  const unsigned xdst = lds0 + C::XOFF + w * 1024;
  auto dma_w = [&](int d, int stage) {
    const char* b = wbase + (size_t)stage * (32 * WSTRIDE);
    if (ROLE == 0) dma16(b, wof[d], wdst + (stage & 3) * C::WSTAGE + d * 1024);
    else dma16m(b, wof[d], wdst + (stage & 3) * C::WSTAGE + d * 1024, wmask[d]);
  };
  auto dma_x = [&](int i, int tile, int buf) {
    if (i == 0) dma16m(x + tile * 128, xof[i], xdst + buf * C::XTILE, xmask0);
    else dma16(x + tile * 128, xof[i], xdst + buf * C::XTILE + i * 8192);
  };
  auto issue_w = [&](int stage) {
#pragma unroll
    for (int i = 0; i < C::WI; ++i) dma_w(i, stage);
  };
  auto issue_x = [&](int odd, int tile) {
#pragma unroll
    for (int i = (odd ? C::XE : 0); i < (odd ? XI : C::XE); ++i) dma_x(i, tile, tile);
  };
  issue_x(0, 0);
  issue_w(0); issue_x(1, 0);
  issue_w(1); issue_x(0, 1);
  const u32x4 ones4 = {0x3F803F80u, 0x3F803F80u, 0x3F803F80u, 0x3F803F80u};
  if (tid < 24) *(LDS_AS u32x4*)(uintptr_t)(lds0 + C::XOFF + (tid >> 3) * C::XTILE + (tid & 7) * 16) = ones4;
  if (ROLE == 1 && tid >= 256) {
    const int t2 = tid - 256, row = (t2 >> 1) & 31;
    const int f = (row & 3) | ((row >> 1) & 4);
    *(LDS_AS u32x4*)(uintptr_t)(lds0 + (t2 >> 6) * C::WSTAGE + row * 512 + (15 ^ f) * 32 + (t2 & 1) * 16) = ones4;
  }

  constexpr int NWA = ROLE == 0 ? 1 : 4;
  unsigned wad[NWA], xad[MT];
  if (ROLE == 0) {
    const int f = ((q >> 1) & 1) | ((g & 1) << 1);
    wad[0] = lds0 + (8 * g + q) * 384 + ((wn ^ f) * 32) + p * 8;
  } else {
    const int f = q | ((g & 1) << 2);
#pragma unroll
    for (int e = 0; e < NWA; ++e) wad[e] = lds0 + (8 * g + q) * 512 + (((wn * 4 + e) ^ f) * 32) + p * 8;
  }
  int lrow[MT];
#pragma unroll
  for (int j = 0; j < MT; ++j) {
    const int act = ROLE == 0 ? ((j + wn) & 3) : (wn ? 2 - j : j);
    lrow[j] = (wm * MT + act) * 16 + m;
    xad[j] = lds0 + C::XOFF + lrow[j] * 128 + ((g ^ ((lrow[j] >> 1) & 7)) * 16);
  }
  f32x4 acc[MT][NT];
#pragma unroll
  for (int j = 0; j < MT; ++j)
#pragma unroll
    for (int n = 0; n < NT; ++n) acc[j][n] = (f32x4){0.f, 0.f, 0.f, 0.f};
  f32x4 st0 = {0.f, 0.f, 0.f, 0.f}, st1 = {0.f, 0.f, 0.f, 0.f};
  const bf8 onesf = __builtin_bit_cast(bf8, ones4);
  bf8 xf[2][MT];
  s16x4 wh[2][NT][2];

  auto read_one = [&](int par, int r, unsigned woff, unsigned xoff) {
    if (r < MT) {
      xf[par][r] = lds_b128((xad[r] + xoff) ^ (par ? 64u : 0u));
    } else {
      const int n = (r - MT) >> 1, hl = (r - MT) & 1;
      if (ROLE == 0) wh[par][n][hl] = tr4(wad[0] + woff + (n & 1) * 12288 + (n >> 1) * 128 + hl * 4 * 384);
      else wh[par][n][hl] = tr4(wad[n & 3] + woff + (n >> 2) * 256 + hl * 4 * 512);
    }
  };
  constexpr int NR = MT + 2 * NT;

  wait_vm_lgkm0<n_issue<ROLE>(-3)>();
  __builtin_amdgcn_s_barrier();
#pragma unroll
  for (int r = 0; r < NR; ++r) read_one(0, r, 0, 0);
  STAMP(1);

  int xr = 0;
#define JOB_HOOK
#define ITER(T, PAR, VM, DOW, DOX, DORD, DBL)                                                    \
  {                                                                                          \
    if (DORD) { wait_vm_lgkm0<VM>(); __builtin_amdgcn_s_barrier(); }                         \
    SB0;                                                                                     \
    const int xn = xr == 2 ? 0 : xr + 1;                                                     \
    const int xq = xn == 2 ? 0 : xn + 1;                                                     \
    const unsigned woff = (((T) + 1) & 3) * C::WSTAGE, xoff = (PAR ? xn : xr) * C::XTILE;    \
    _Pragma("unroll") for (int i = 0; i < 24; ++i) {                                         \
      const int n = i / MT, j = i % MT;                                                      \
      acc[j][n] = mfma(cat8(wh[PAR][n][0], wh[PAR][n][1]), xf[PAR][j], acc[j][n]);           \
      if (DORD && i < 20 && i * NR / 20 != (i + 1) * NR / 20) read_one(PAR ^ 1, i * NR / 20, woff, xoff); \
      if (DBL && i % 3 == 2) {     \
        const int V = 2 * (PAR) - 2 + (i / 12), d = (i / 3) & 3, pc_ = V + 5;                \
        if (d < C::WI) dma_w(d, V + 4);                                                      \
        else {                                                                               \
          const int xi = ((pc_ & 1) ? C::XE : 0) + (d - C::WI);                              \
          if (xi < ((pc_ & 1) ? XI : C::XE)) dma_x(xi, pc_ >> 1, (pc_ >> 1) % 3);            \
        }                                                                                    \
      }                                                                                      \
      if (!DBL && DOW && i % 6 == 2) {                                                       \
        const int d = i / 6;                                                                 \
        if (d < C::WI) dma_w(d, (T) + 4);                                                    \
        else if (DOX) {                                                                      \
          const int xi = (PAR ? 0 : C::XE) + (d - C::WI);                                    \
          if (xi < (PAR ? C::XE : XI)) dma_x(xi, ((T) + 5) >> 1, PAR ? xr : xq);             \
        }                                                                                    \
      }                                                                                      \
      JOB_HOOK                                                                               \
      if (i == 21) st0 = mfma(xf[PAR][0], xf[PAR][0], st0);                                  \
      if (i == 22) {                                                                         \
        if (ROLE == 0) st1 = mfma(onesf, xf[PAR][0], st1);                                   \
        else if (wn == PAR) st1 = mfma(xf[PAR][1], xf[PAR][1], st1);                         \
      }                                                                                      \
      SB0;                                                                                   \
    }                                                                                        \
    if (PAR) xr = xn;                                                                        \
  }
  constexpr int VMF = n_issue<ROLE>(0) + n_issue<ROLE>(1);
  ITER(0, 0, 0, true, true, true, true)
  ITER(1, 1, n_issue<ROLE>(-1), true, true, true, true)
  for (int t = 2; t < 14; t += 2) {
    ITER(t, 0, VMF, true, true, true, false)
    ITER(t + 1, 1, VMF, true, true, true, false)
  }
  ITER(14, 0, VMF, true, true, true, false)
  ITER(15, 1, VMF, true, false, true, false)
  ITER(16, 0, n_issue<ROLE>(14) + n_issue<ROLE>(15), false, false, true, false)
  ITER(17, 1, n_issue<ROLE>(15), false, false, true, false)
  unsigned jl[8];
  const int J = job + w;
  const char* jsrc = job_src(wo, wd, J);
#undef JOB_HOOK
#define JOB_HOOK if (ROLE == 0 && i < 8) jl[i] = *(const unsigned*)(jsrc + (unsigned)((8 * g + i) * 1280 + m * 4));
  ITER(18, 0, 0, false, false, true, false)
#undef JOB_HOOK
#define JOB_HOOK
  ITER(19, 1, 0, false, false, false, false)
#undef ITER
#undef JOB_HOOK
  STAMP(2);

  LDS_AS float* rsum = (LDS_AS float*)(uintptr_t)(lds0 + STAT_OFF);
  LDS_AS float* rsq = rsum + 192;
  LDS_AS float* csum = rsum + 448;
  {
    const bool diag = g == q;
    const unsigned e0 = p == 0 ? ~0u : 0u, e1 = p == 1 ? ~0u : 0u, e2 = p == 2 ? ~0u : 0u, e3 = p == 3 ? ~0u : 0u;
    const float d0 = fbits((ubits(st0[0]) & e0) | (ubits(st0[1]) & e1) | (ubits(st0[2]) & e2) | (ubits(st0[3]) & e3));
    if (ROLE == 0) {
      if (diag) { rsq[lrow[0]] = d0; rsum[lrow[0]] = st1[0]; }
    } else {
      const float d1 = fbits((ubits(st1[0]) & e0) | (ubits(st1[1]) & e1) | (ubits(st1[2]) & e2) | (ubits(st1[3]) & e3));
      if (diag) {
        rsq[lrow[0]] = d0;
        rsq[wn ? 192 + wm * 16 + m : lrow[1]] = d1;
        if (wn) {
#pragma unroll
          for (int j = 0; j < MT; ++j) rsum[lrow[j]] = acc[j][7][0];
        }
      }
    }
  }
  if (wm == 0) {
#pragma unroll
    for (int j = 0; j < MT; ++j) {
      if (lrow[j] == 0) {
#pragma unroll
        for (int n = 0; n < NT; ++n) *(LDS_AS f32x4*)(csum + (wn * NT + n) * 16 + 4 * g) = acc[j][n];
      }
    }
  }
  lds_barrier();
  float ra[MT], rbv[MT], ir2[MT], k1[MT], k3[MT];
#pragma unroll
  for (int j = 0; j < MT; ++j) {
    const float mean = rsum[lrow[j]] * (1.f / D_);
    float sq = rsq[lrow[j]];
    if (ROLE == 1 && j == 1) sq += rsq[192 + wm * 16 + m];
    const float var = fmaxf(sq * (1.f / D_) - mean * mean, 0.f);
    ra[j] = __builtin_amdgcn_rsqf(var + 1e-5f);
    rbv[j] = ROLE == 0 ? -mean : -mean * ra[j];
    ir2[j] = var + 1e-5f;
    k1[j] = -2.3022082f * ra[j];
    k3[j] = -0.10294324f * ra[j] * ra[j] * ra[j];
  }
  STAMP(3);
  if (ROLE == 0) job_store(B2, J, true, jl, g, m);
  const rsrc_t a2r = mk_rsrc(A2, (unsigned)A2_BYTES);
  if (ROLE == 0) {
    unsigned so[2];
#pragma unroll
    for (int jp = 0; jp < 2; ++jp) {
      const int lr = (wm * 4 + ((2 * jp + (g & 1) + wn) & 3)) * 16 + m;
      const int gr = rbase + lr;
      so[jp] = (lr >= 1 && gr < S_) ? a2_off(gr, 640 + cg * 192 + wn * 16 + (g >> 1) * 8) : OOB;
    }
#pragma unroll
    for (int a = 0; a < 3; ++a) {
      const f32x4 cg4 = *(LDS_AS f32x4*)(csum + (wn * NT + 2 * a) * 16 + 4 * g);
      const f32x4 cu4 = *(LDS_AS f32x4*)(csum + (wn * NT + 2 * a + 1) * 16 + 4 * g);
#pragma unroll
      for (int jp = 0; jp < 2; ++jp) {
        unsigned hp[2][2];
#pragma unroll
        for (int jj = 0; jj < 2; ++jj) {
          const int j = 2 * jp + jj;
#pragma unroll
          for (int r = 0; r < 2; ++r) {
            const f32x2 ag = {acc[j][2 * a][2 * r], acc[j][2 * a][2 * r + 1]};
            const f32x2 au = {acc[j][2 * a + 1][2 * r], acc[j][2 * a + 1][2 * r + 1]};
            const f32x2 cgv = {cg4[2 * r], cg4[2 * r + 1]}, cuv = {cu4[2 * r], cu4[2 * r + 1]};
            const f32x2 tg = cgv * rbv[j] + ag;
            const f32x2 tu = cuv * rbv[j] + au;
            const f32x2 z = tg * (tg * tg * k3[j] + k1[j]);
            const f32x2 gu = tg * tu;
            const f32x2 ev = {__builtin_amdgcn_exp2f(z[0]), __builtin_amdgcn_exp2f(z[1])};
            const f32x2 dn = ev * ir2[j] + ir2[j];
            const float h0 = gu[0] * __builtin_amdgcn_rcpf(dn[0]);
            const float h1 = gu[1] * __builtin_amdgcn_rcpf(dn[1]);
            hp[jj][r] = pk2(h0, h1);
          }
        }
        auto s0 = __builtin_amdgcn_permlane16_swap(hp[0][0], hp[1][0], false, false);
        auto s1 = __builtin_amdgcn_permlane16_swap(hp[0][1], hp[1][1], false, false);
        bst128(a2r, so[jp] + a * 8192, (u32x4){s0[0], s1[0], s0[1], s1[1]});
      }
    }
  } else {
#pragma unroll
    for (int n = 0; n < NT; ++n) {
      const int tau = (n >> 2) * 8 + wn * 4 + (n & 3);
      const f32x4 c4 = *(LDS_AS f32x4*)(csum + (wn * NT + n) * 16 + 4 * g);
      if (tau < 15) {
#pragma unroll
        for (int j = 0; j < MT; ++j) {
          const f32x4 v = acc[j][n] * ra[j] + c4 * rbv[j];
          *(LDS_AS u32x2*)(uintptr_t)(lds0 + lrow[j] * QRS + tau * 32 + g * 8) = (u32x2){pk2(v[0], v[1]), pk2(v[2], v[3])};
        }
      }
    }
    lds_barrier();
    STAMP(4);
    const float sc = 0.22360679775f * 1.44269504089f;
    constexpr int NI = 6;
    const int h = w & 3, mt0 = 1 + (w >> 2);
    const bool odd = m & 1;
    const bool lea = 4 * g + (m & 1) <= m, leb = 4 * g + 2 + (m & 1) <= m;
    const unsigned mpar = odd ? 0xffff0000u : 0x0000ffffu;
    unsigned m0a = lea ? 0u : mpar, m1a = lea ? mpar : 0u, m0b = leb ? 0u : mpar, m1b = leb ? mpar : 0u;
    unsigned qma = g == 3 ? 0u : ~0u, qmb = g >= 2 ? 0u : ~0u;
    asm volatile("" : "+v"(m0a), "+v"(m1a), "+v"(m0b), "+v"(m1b), "+v"(qma), "+v"(qmb));
    const unsigned rq = lds0 + ((mt0 - 1) * 16 + m) * QRS + h * 40 + g * 16;
    const unsigned va = lds0 + ((mt0 - 1) * 16 + 4 * g + q) * QRS + 160 + h * 40 + p * 8;
    unsigned rq2 = rq + 96 * QRS, va2 = va + 96 * QRS;
    asm volatile("" : "+v"(rq2), "+v"(va2));
    const int kq = cg * 80 + h * 20;
    const unsigned vo0 = a2_off(m, kq + 4 * g), vo1 = g == 0 ? a2_off(m, kq + 16) : OOB;
    bf8 v0[NI], v1[NI];
    f32x4 s0[NI], s1[NI];
    const f32x4 z4 = {0.f, 0.f, 0.f, 0.f};
#pragma unroll
    for (int ii = 0; ii < NI; ++ii) {
      const unsigned r0 = (ii < 3 ? rq : rq2) + (ii % 3) * 32 * QRS, vv = (ii < 3 ? va : va2) + (ii % 3) * 32 * QRS;
      const u32x2 k0a = lds_b64(r0), k0b = lds_b64(r0 + 8);
      const u32x2 k1a = lds_b64(r0 + 16 * QRS), k1b = lds_b64(r0 + 16 * QRS + 8);
      const u32x2 qa = lds_b64(r0 + 16 * QRS + 320), qb = lds_b64(r0 + 16 * QRS + 328);
      v0[ii] = cat8(tr4(vv), tr4(vv + 16 * QRS));
      v1[ii] = cat8(tr4(vv + 32), tr4(vv + 16 * QRS + 32));
      const bf8 qf = __builtin_bit_cast(bf8, (u32x4){qa[0] & qma, qa[1] & qma, qb[0] & qmb, qb[1] & qmb});
      s0[ii] = mfma(__builtin_bit_cast(bf8, (u32x4){k0a[0], k0a[1], k0b[0], k0b[1]}), qf, z4);
      s1[ii] = mfma(__builtin_bit_cast(bf8, (u32x4){k1a[0], k1a[1], k1b[0], k1b[1]}), qf, z4);
    }
    u32x2 oa[NI], ob[NI];
    const unsigned ev = odd ? 0u : ~0u, od = ~ev, sa = lea ? ~0u : 0u, sb = leb ? ~0u : 0u;
    const bool bad = rbase + (mt0 - 1) * 16 < 0;
    const unsigned bada = (bad && !lea) ? ~0u : 0u, badb = (bad && !leb) ? ~0u : 0u;
    unsigned k00 = ev & ~sa, k01 = od & ~sa, k10 = ev & sa, k11 = od & sa;
    unsigned k02 = ev & ~sb, k03 = od & ~sb, k12 = ev & sb, k13 = od & sb;
    asm volatile("" : "+v"(k00), "+v"(k01), "+v"(k10), "+v"(k11), "+v"(k02), "+v"(k03), "+v"(k12), "+v"(k13));
    float la[NI], lb[NI], mx[NI];
#pragma unroll
    for (int ii = 0; ii < NI; ++ii) {
      unsigned ua = (ubits(s0[ii][0]) & k00) | (ubits(s0[ii][1]) & k01) | (ubits(s1[ii][0]) & k10) | (ubits(s1[ii][1]) & k11);
      unsigned ub = (ubits(s0[ii][2]) & k02) | (ubits(s0[ii][3]) & k03) | (ubits(s1[ii][2]) & k12) | (ubits(s1[ii][3]) & k13);
      if (ii == 0) {
        ua = (ua & ~bada) | (0xff7fffffu & bada);
        ub = (ub & ~badb) | (0xff7fffffu & badb);
      }
      la[ii] = fbits(ua); lb[ii] = fbits(ub);
      mx[ii] = vmax(la[ii], lb[ii]);
    }
#pragma unroll
    for (int ii = 0; ii < NI; ++ii) mx[ii] = red_max(mx[ii]) * sc;
#pragma unroll
    for (int ii = 0; ii < NI; ++ii) {
      la[ii] = __builtin_amdgcn_exp2f(la[ii] * sc - mx[ii]);
      lb[ii] = __builtin_amdgcn_exp2f(lb[ii] * sc - mx[ii]);
      mx[ii] = la[ii] + lb[ii];
    }
#pragma unroll
    for (int ii = 0; ii < NI; ++ii) mx[ii] = __builtin_amdgcn_rcpf(red_sum(mx[ii]));
#pragma unroll
    for (int ii = 0; ii < NI; ++ii) {
      const float pa_ = la[ii] * mx[ii], pb_ = lb[ii] * mx[ii];
      const unsigned pa = pk2(pa_, pa_), pb = pk2(pb_, pb_);
      const bf8 pf = __builtin_bit_cast(bf8, (u32x4){pa & m0a, pb & m0b, pa & m1a, pb & m1b});
      s0[ii] = mfma(v0[ii], pf, z4);
      s1[ii] = mfma(v1[ii], pf, z4);
    }
#pragma unroll
    for (int ii = 0; ii < NI; ++ii) {
      oa[ii] = (u32x2){pk2(s0[ii][0], s0[ii][1]), pk2(s0[ii][2], s0[ii][3])};
      ob[ii] = (u32x2){pk2(s1[ii][0], s1[ii][1]), pk2(s1[ii][2], s1[ii][3])};
    }
#pragma unroll
    for (int ii = 0; ii < NI; ++ii) {
      const int mt = mt0 + 2 * ii, T = rb * 11 - 1 + mt;
      if (mt < 12 && T < 128) {
        const int so = (T >> 2) * (KS2 * 4096) + (T & 3) * 1024;
        __builtin_amdgcn_raw_buffer_store_b64(oa[ii], a2r, vo0, so, ST_AUX);
        __builtin_amdgcn_raw_buffer_store_b64(ob[ii], a2r, vo1, so, ST_AUX);
      }
    }
  }
  STAMP(5);
}

__global__ __launch_bounds__(512, 2) void phase1(const char* __restrict__ x, const char* __restrict__ wqkv,
                                                 const char* __restrict__ wo, const char* __restrict__ wg,
                                                 const char* __restrict__ wu, const char* __restrict__ wd,
                                                 char* __restrict__ A2, char* __restrict__ B2, char* __restrict__ out) {
  __shared__ __attribute__((aligned(1024))) char smem[LDS1_BYTES];
  const unsigned lds0 = (unsigned)(uintptr_t)(LDS_AS char*)smem;
  const int b = blockIdx.x, xcd = b & 7, slot = b >> 3;
  if (slot >= 12 && slot < 16) {
    unsigned long long pc0;
    int dlen;
    asm volatile("s_getpc_b64 %0\nP1_REF_LBL:\n\ts_mov_b32 %1, P2_TAIL_LBL-P1_REF_LBL" : "=s"(pc0), "=s"(dlen));
    const unsigned long long lo = pc0 & ~15ull;
    const unsigned len = dlen > 0 ? (unsigned)dlen : 0u;
    unsigned o = ((slot - 12) * 512 + threadIdx.x) * 16;
    unsigned sink = 0;
    for (; o + 16 <= len && o < 131072u; o += 32768) sink ^= (*(const u32x4*)((const char*)lo + o))[0];
    const rsrc_t tr = slot == 12 ? mk_rsrc(A2, (unsigned)A2_BYTES + 8 * KS2 * 5120)
                    : slot == 13 ? mk_rsrc(out, S_ * D_ * 2)
                    : slot == 14 ? mk_rsrc((void*)wd, F_ * D_ * 2) : mk_rsrc((void*)wo, D_ * D_ * 2);
    sink ^= __builtin_amdgcn_raw_buffer_load_b32(tr, threadIdx.x << (slot == 12 ? 15 : 13), 0, 17);
    asm volatile("" ::"v"(sink));
  }
  if (slot < 12) {
    const int hg = 2 * (xcd & 3) + slot / 6, rb = 6 * (xcd >> 2) + slot % 6;
    phase1_body<1>(x, wqkv, wqkv, wo, wd, A2, B2, rb, hg, lds0, 0);
  } else {
    const int L = xcd * 20 + (slot - 12);
    if (L >= 153) {
      const int lane = threadIdx.x & 63, w = __builtin_amdgcn_readfirstlane(threadIdx.x >> 6), g = lane >> 4, m = lane & 15;
      unsigned jl[5][8];
#pragma unroll
      for (int r = 0; r < 5; ++r) {
        const int J = 1224 + r * 56 + (L - 153) * 8 + w;
        const char* jsrc = job_src(wo, wd, J < 1480 ? J : J - 1480);
#pragma unroll
        for (int i = 0; i < 8; ++i) jl[r][i] = *(const unsigned*)(jsrc + (unsigned)((8 * g + i) * 1280 + m * 4));
      }
#pragma unroll
      for (int r = 0; r < 5; ++r) {
        const int J = 1224 + r * 56 + (L - 153) * 8 + w;
        job_store(B2, J < 1480 ? J : J - 1480, J < 1480, jl[r], g, m);
      }
      return;
    }
    int rb, cg;
    if (L < 72) { cg = L >> 3; rb = L & 7; } else { cg = (L - 72) / 9; rb = 8 + (L - 72) % 9; }
    phase1_body<0>(x, wg, wu, wo, wd, A2, B2, rb, cg, lds0, L * 8);
  }
}

__global__ __launch_bounds__(256, 1) void phase2(const char* __restrict__ A2, const char* __restrict__ B2,
                                                 const char* __restrict__ x, char* __restrict__ out) {
  __shared__ __attribute__((aligned(16))) f32x4 red[4][3][5][64];
  const int tid = threadIdx.x, lane = tid & 63;
  const int w = __builtin_amdgcn_readfirstlane(tid >> 6);
  const int g = lane >> 4, m = lane & 15;
  const int b = blockIdx.x, xcd = b & 7, slot = b >> 3;
  const int rb = (xcd >> 1) * 8 + (slot & 7), cg = (xcd & 1) * 4 + (slot >> 3);
  const int pw = ((w & 1) << 1) | (w >> 1);
  const char* ap = A2 + (size_t)(rb * KS2) * 4096;
  const char* bp = B2 + (size_t)(cg * KS2) * 5120;
  const unsigned lo = lane * 16;
  unsigned la[4];
#pragma unroll
  for (int j = 0; j < 4; ++j) la[j] = lo + (j ^ pw) * 1024;
  STAMP2(0);
  constexpr int DEP = P2DEP, NI = 19;
  u32x4 af[DEP][4], bfr[DEP][5];
  f32x4 acc[4][5];
#pragma unroll
  for (int i = 0; i < 4; ++i)
#pragma unroll
    for (int n = 0; n < 5; ++n) acc[i][n] = (f32x4){0.f, 0.f, 0.f, 0.f};
  auto load1 = [&](int i, int u) {
    const int ks = i == NI - 1 ? 72 + (w >> 1) : w + 4 * i;
    if (u < 5) bfr[i % DEP][u] = *(const u32x4*)(bp + ks * 5120 + (lo + u * 1024));
    else if (i < NI - 1 || u < 7) af[i % DEP][u - 5] = *(const u32x4*)(ap + ks * 4096 + la[u - 5]);
  };
  const int orow = rb * 64 + pw * 16 + m;
  const size_t ooff = (size_t)orow * 1280 + (cg * 80 + 4 * g) * 2;
  u32x2 xr[5];
#pragma unroll
  for (int i = 0; i < DEP - 1; ++i)
#pragma unroll
    for (int u = 0; u < 9; ++u) load1(i, u);
#pragma unroll
  for (int n = 0; n < 5; ++n) xr[n] = *(const u32x2*)(x + ooff + n * 32);
  SB0;
#pragma unroll
  for (int i = 0; i < NI; ++i) {
#pragma unroll
    for (int u = 0; u < (i == NI - 1 ? 10 : 20); ++u) {
      const int j = u / 5, n = u % 5;
      acc[j][n] = mfma(__builtin_bit_cast(bf8, bfr[i % DEP][n]), __builtin_bit_cast(bf8, af[i % DEP][j]), acc[j][n]);
      if ((u & 1) == 0 && u < 18 && i + DEP - 1 < NI) load1(i + DEP - 1, u >> 1);
      SB0;
    }
    if (i == NI - 2) {
#pragma unroll
      for (int j = 2; j < 4; ++j)
#pragma unroll
        for (int n = 0; n < 5; ++n) red[j ^ pw][j - 1][n][lane] = acc[j][n];
    }
  }
  STAMP2(1);
#pragma unroll
  for (int n = 0; n < 5; ++n) red[1 ^ pw][0][n][lane] = acc[1][n];
  __syncthreads();
#pragma unroll
  for (int n = 0; n < 5; ++n) {
    const f32x4 s = (acc[0][n] + red[pw][0][n][lane]) + (red[pw][1][n][lane] + red[pw][2][n][lane]);
    const float x0 = fbits(xr[n][0] << 16), x1 = fbits(xr[n][0] & 0xffff0000u);
    const float x2 = fbits(xr[n][1] << 16), x3 = fbits(xr[n][1] & 0xffff0000u);
    *(u32x2*)(out + ooff + n * 32) = (u32x2){pk2(s[0] + x0, s[1] + x1), pk2(s[2] + x2, s[3] + x3)};
  }
  asm volatile("P2_TAIL_LBL:");
  STAMP2(2);
}

extern "C" void kernel_launch(void* const* d_in, const int* in_sizes, int n_in, void* d_out, int out_size, void* d_ws,
                              size_t ws_size, hipStream_t stream) {
  const char* x = (const char*)d_in[0];
  const char* wqkv = (const char*)d_in[1];
  const char* wo = (const char*)d_in[2];
  const char* wg = (const char*)d_in[4];
  const char* wu = (const char*)d_in[5];
  const char* wd = (const char*)d_in[6];
  char* A2 = (char*)d_ws;
  char* B2 = A2 + A2_BYTES;
  phase1<<<256, 512, 0, stream>>>(x, wqkv, wo, wg, wu, wd, A2, B2, (char*)d_out);
  phase2<<<256, 256, 0, stream>>>(A2, B2, x, (char*)d_out);
}
```

```cpp
#include <hip/hip_runtime.h>
#include <hip/hip_bf16.h>
#include <stdint.h>

typedef __attribute__((ext_vector_type(4))) short s16x4;
typedef __attribute__((ext_vector_type(8))) short s16x8;
typedef __attribute__((ext_vector_type(2))) __bf16 bf2;
typedef __attribute__((ext_vector_type(8))) __bf16 bf8;
typedef __attribute__((ext_vector_type(4))) float f32x4;
typedef __attribute__((ext_vector_type(4))) unsigned u32x4;
typedef __attribute__((ext_vector_type(2))) unsigned u32x2;
#define LDS_AS __attribute__((address_space(3)))
#define DEVI __device__ __forceinline__

constexpr int S_ = 2048, D_ = 640, F_ = 1728;
constexpr int KS2 = 74;
constexpr size_t A2_BYTES = (size_t)32 * KS2 * 4096;
constexpr int STAT_OFF = 147456;
constexpr int LDS1_BYTES = STAT_OFF + 3328;
constexpr int QRS = 488;

#define STAMP(i)
#define STAMP2(i)
template <int ROLE> struct Cfg;
template <> struct Cfg<0> {
  static constexpr int WM = 2, WN = 4, MT = 4, NT = 6, WROW = 384, WSTAGE = 24576, XTILE = 16384,
                       XOFF = 98304, WI = 3, XE = 1, XO = 1;
};
template <> struct Cfg<1> {
  static constexpr int WM = 4, WN = 2, MT = 3, NT = 8, WROW = 512, WSTAGE = 16384, XTILE = 24576,
                       XOFF = 65536, WI = 2, XE = 2, XO = 1;
};
typedef __attribute__((ext_vector_type(2))) float f32x2;

DEVI void dma16(const char* sbase, unsigned voff, unsigned lds_off) {
  asm volatile("s_mov_b32 m0, %0\n\ts_nop 0\n\tglobal_load_lds_dwordx4 %1, %2" ::"s"(lds_off), "v"(voff), "s"(sbase) : "memory");
}
DEVI void dma16m(const char* sbase, unsigned voff, unsigned lds_off, unsigned long long mask) {
  asm volatile("s_mov_b32 m0, %0\n\ts_mov_b64 exec, %3\n\tglobal_load_lds_dwordx4 %1, %2\n\ts_mov_b64 exec, -1"
               ::"s"(lds_off), "v"(voff), "s"(sbase), "s"(mask) : "memory");
}
#define SB0 __builtin_amdgcn_sched_barrier(0)
template <int N> DEVI void wait_vm_lgkm0() {
  asm volatile("" ::: "memory");
  __builtin_amdgcn_s_waitcnt((N & 15) | ((N >> 4) << 14) | (7 << 4));
  asm volatile("" ::: "memory");
}
DEVI void lds_barrier() {
  asm volatile("" ::: "memory");
  __builtin_amdgcn_s_waitcnt(0xC07F);
  __builtin_amdgcn_s_barrier();
  asm volatile("" ::: "memory");
}
DEVI s16x4 tr4(unsigned a) { return __builtin_amdgcn_ds_read_tr16_b64_v4i16((LDS_AS s16x4*)(uintptr_t)a); }
DEVI bf8 cat8(s16x4 lo, s16x4 hi) { return __builtin_bit_cast(bf8, __builtin_shufflevector(lo, hi, 0, 1, 2, 3, 4, 5, 6, 7)); }
DEVI bf8 lds_b128(unsigned a) { return __builtin_bit_cast(bf8, *(LDS_AS u32x4*)(uintptr_t)a); }
DEVI u32x2 lds_b64(unsigned a) { return *(LDS_AS u32x2*)(uintptr_t)a; }
DEVI unsigned pk2(float a, float b) {
  bf2 v = {(__bf16)a, (__bf16)b};
  return __builtin_bit_cast(unsigned, v);
}
DEVI f32x4 mfma(bf8 a, bf8 b, f32x4 c) { return __builtin_amdgcn_mfma_f32_16x16x32_bf16(a, b, c, 0, 0, 0); }
DEVI float fbits(unsigned u) { return __builtin_bit_cast(float, u); }
DEVI unsigned ubits(float f) { return __builtin_bit_cast(unsigned, f); }
#define P2DEP 4
#define ST_AUX 16
typedef __amdgpu_buffer_rsrc_t rsrc_t;
DEVI rsrc_t mk_rsrc(void* p, unsigned bytes) { return __builtin_amdgcn_make_buffer_rsrc(p, 0, bytes, 0x00020000); }
DEVI void bst64(rsrc_t r, unsigned off, u32x2 v) { __builtin_amdgcn_raw_buffer_store_b64(v, r, off, 0, ST_AUX); }
DEVI void bst128(rsrc_t r, unsigned off, u32x4 v) { __builtin_amdgcn_raw_buffer_store_b128(v, r, off, 0, ST_AUX); }
constexpr unsigned OOB = 0x80000000u;
DEVI float vmax(float a, float b) { float r; asm("v_max_f32 %0, %1, %2" : "=v"(r) : "v"(a), "v"(b)); return r; }
DEVI float red_max(float v) {
  auto a = __builtin_amdgcn_permlane16_swap(ubits(v), ubits(v), false, false);
  v = vmax(fbits(a[0]), fbits(a[1]));
  auto b = __builtin_amdgcn_permlane32_swap(ubits(v), ubits(v), false, false);
  return vmax(fbits(b[0]), fbits(b[1]));
}
DEVI float red_sum(float v) {
  auto a = __builtin_amdgcn_permlane16_swap(ubits(v), ubits(v), false, false);
  v = fbits(a[0]) + fbits(a[1]);
  auto b = __builtin_amdgcn_permlane32_swap(ubits(v), ubits(v), false, false);
  return fbits(b[0]) + fbits(b[1]);
}
DEVI unsigned a2_off(int r, int k) {
  return (unsigned)(((r >> 6) * KS2 + (k >> 5)) * 4096 + ((r >> 4) & 3) * 1024 + ((((k >> 3) & 3) * 16 + (r & 15)) * 16) +
                    ((k >> 2) & 1) * 8);
}
template <int ROLE> constexpr int n_issue(int t) {
  return (t <= 15 ? Cfg<ROLE>::WI : 0) + (t <= 14 ? (((t + 5) & 1) ? Cfg<ROLE>::XO : Cfg<ROLE>::XE) : 0);
}

DEVI const char* job_src(const char* wo, const char* wd, int J) {
  const int jks = J / 20, jcb = J % 20;
  return (jks < 20 ? wo + (size_t)(jks * 32) * 1280 : wd + (size_t)(jks * 32 - 640) * 1280) + jcb * 64;
}
DEVI void job_store(char* B2, int J, bool ok, const unsigned* jl, int g, int m) {
  const int jks = J / 20, jcb = J % 20;
  const int n0 = jcb * 32 + 2 * m, nt = n0 >> 4;
  const rsrc_t b2r = mk_rsrc(B2, 8 * KS2 * 5120);
  const unsigned dst = ok ? (unsigned)((((nt / 5) * KS2 + jks) * 5 + nt % 5) * 1024 + (g * 16 + (n0 & 15)) * 16) : OOB;
#pragma unroll
  for (int i = 0; i < 2; ++i) {
    u32x4 o;
#pragma unroll
    for (int j = 0; j < 4; ++j) o[j] = __builtin_amdgcn_perm(jl[2 * j + 1], jl[2 * j], i ? 0x07060302u : 0x05040100u);
    bst128(b2r, dst + i * 16, o);
  }
}

template <int ROLE>
DEVI void phase1_body(const char* __restrict__ x, const char* __restrict__ wa, const char* __restrict__ wb,
                      const char* __restrict__ wo, const char* __restrict__ wd, char* __restrict__ A2,
                      char* __restrict__ B2, int rb, int cg, unsigned lds0, int job) {
  using C = Cfg<ROLE>;
  constexpr int MT = C::MT, NT = C::NT;
  const int tid = threadIdx.x, lane = tid & 63;
  const int w = __builtin_amdgcn_readfirstlane(tid >> 6);
  const int g = lane >> 4, m = lane & 15, q = m >> 2, p = lane & 3;
  const int wm = w / C::WN, wn = w % C::WN;
  const int rbase = ROLE == 0 ? rb * 127 - 1 : rb * 176 - 16;
  STAMP(0);

  unsigned wof[C::WI];
  unsigned long long wmask[C::WI];
  constexpr int WSTRIDE = ROLE == 0 ? F_ * 2 : 3 * D_ * 2;
  const char* wbase;
  if (ROLE == 0) {
    wbase = (w < 4 ? wa : wb) + cg * 384;
#pragma unroll
    for (int i = 0; i < C::WI; ++i) {
      const int idx = ((w & 3) * 3 + i) * 64 + lane;
      const int row = idx / 24, pc = idx % 24;
      const int f = ((row >> 1) & 1) | ((row >> 2) & 2);
      wof[i] = row * WSTRIDE + (((pc >> 1) ^ f) * 32) + (pc & 1) * 16;
    }
  } else {
    wbase = wa + cg * 160;
#pragma unroll
    for (int i = 0; i < C::WI; ++i) {
      const int row = 4 * w + 2 * i + (lane >> 5), pc = lane & 31;
      const int f = (row & 3) | ((row >> 1) & 4);
      const int c = 2 * ((pc >> 1) ^ f) + (pc & 1);
      const int part = c / 10, cc = c % 10;
      const int poff = part == 0 ? 1280 : (part == 1 ? 2560 : 0);
      wof[i] = row * WSTRIDE + poff + cc * 16;
      wmask[i] = __ballot(c < 30);
    }
  }
  constexpr int XI = C::XE + C::XO;
  unsigned xof[XI];
#pragma unroll
  for (int i = 0; i < XI; ++i) {
    const int lr = (i * 8 + w) * 8 + (lane >> 3);
    int gr = rbase + lr;
    gr = gr < 0 ? 0 : (gr > S_ - 1 ? S_ - 1 : gr);
    xof[i] = gr * 1280 + ((lane & 7) ^ ((lr >> 1) & 7)) * 16;
  }
  const unsigned long long xmask0 = w == 0 ? ~0xffull : ~0ull;
  const unsigned wdst = lds0 + w * (C::WI * 1024);
  const unsigned xdst = lds0 + C::XOFF + w * 1024;
  auto dma_w = [&](int d, int stage) {
    const char* b = wbase + (size_t)stage * (32 * WSTRIDE);
    if (ROLE == 0) dma16(b, wof[d], wdst + (stage & 3) * C::WSTAGE + d * 1024);
    else dma16m(b, wof[d], wdst + (stage & 3) * C::WSTAGE + d * 1024, wmask[d]);
  };
  auto dma_x = [&](int i, int tile, int buf) {
    if (i == 0) dma16m(x + tile * 128, xof[i], xdst + buf * C::XTILE, xmask0);
    else dma16(x + tile * 128, xof[i], xdst + buf * C::XTILE + i * 8192);
  };
  auto issue_w = [&](int stage) {
#pragma unroll
    for (int i = 0; i < C::WI; ++i) dma_w(i, stage);
  };
  auto issue_x = [&](int odd, int tile) {
#pragma unroll
    for (int i = (odd ? C::XE : 0); i < (odd ? XI : C::XE); ++i) dma_x(i, tile, tile);
  };
  issue_x(0, 0);
  issue_w(0); issue_x(1, 0);
  issue_w(1); issue_x(0, 1);
  const u32x4 ones4 = {0x3F803F80u, 0x3F803F80u, 0x3F803F80u, 0x3F803F80u};
  if (tid < 24) *(LDS_AS u32x4*)(uintptr_t)(lds0 + C::XOFF + (tid >> 3) * C::XTILE + (tid & 7) * 16) = ones4;
  if (ROLE == 1 && tid >= 256) {
    const int t2 = tid - 256, row = (t2 >> 1) & 31;
    const int f = (row & 3) | ((row >> 1) & 4);
    *(LDS_AS u32x4*)(uintptr_t)(lds0 + (t2 >> 6) * C::WSTAGE + row * 512 + (15 ^ f) * 32 + (t2 & 1) * 16) = ones4;
  }

  constexpr int NWA = ROLE == 0 ? 1 : 4;
  unsigned wad[NWA], xad[MT];
  if (ROLE == 0) {
    const int f = ((q >> 1) & 1) | ((g & 1) << 1);
    wad[0] = lds0 + (8 * g + q) * 384 + ((wn ^ f) * 32) + p * 8;
  } else {
    const int f = q | ((g & 1) << 2);
#pragma unroll
    for (int e = 0; e < NWA; ++e) wad[e] = lds0 + (8 * g + q) * 512 + (((wn * 4 + e) ^ f) * 32) + p * 8;
  }
  int lrow[MT];
#pragma unroll
  for (int j = 0; j < MT; ++j) {
    const int act = ROLE == 0 ? ((j + wn) & 3) : (wn ? 2 - j : j);
    lrow[j] = (wm * MT + act) * 16 + m;
    xad[j] = lds0 + C::XOFF + lrow[j] * 128 + ((g ^ ((lrow[j] >> 1) & 7)) * 16);
  }
  f32x4 acc[MT][NT];
#pragma unroll
  for (int j = 0; j < MT; ++j)
#pragma unroll
    for (int n = 0; n < NT; ++n) acc[j][n] = (f32x4){0.f, 0.f, 0.f, 0.f};
  f32x4 st0 = {0.f, 0.f, 0.f, 0.f}, st1 = {0.f, 0.f, 0.f, 0.f};
  const bf8 onesf = __builtin_bit_cast(bf8, ones4);
  bf8 xf[2][MT];
  s16x4 wh[2][NT][2];

  auto read_one = [&](int par, int r, unsigned woff, unsigned xoff) {
    if (r < MT) {
      xf[par][r] = lds_b128((xad[r] + xoff) ^ (par ? 64u : 0u));
    } else {
      const int n = (r - MT) >> 1, hl = (r - MT) & 1;
      if (ROLE == 0) wh[par][n][hl] = tr4(wad[0] + woff + (n & 1) * 12288 + (n >> 1) * 128 + hl * 4 * 384);
      else wh[par][n][hl] = tr4(wad[n & 3] + woff + (n >> 2) * 256 + hl * 4 * 512);
    }
  };
  constexpr int NR = MT + 2 * NT;

  wait_vm_lgkm0<n_issue<ROLE>(-3)>();
  __builtin_amdgcn_s_barrier();
#pragma unroll
  for (int r = 0; r < NR; ++r) read_one(0, r, 0, 0);
  STAMP(1);

  int xr = 0;
#define JOB_HOOK
#define ITER(T, PAR, VM, DOW, DOX, DORD, DBL)                                                    \
  {                                                                                          \
    if (DORD) { wait_vm_lgkm0<VM>(); __builtin_amdgcn_s_barrier(); }                         \
    SB0;                                                                                     \
    const int xn = xr == 2 ? 0 : xr + 1;                                                     \
    const int xq = xn == 2 ? 0 : xn + 1;                                                     \
    const unsigned woff = (((T) + 1) & 3) * C::WSTAGE, xoff = (PAR ? xn : xr) * C::XTILE;    \
    _Pragma("unroll") for (int i = 0; i < 24; ++i) {                                         \
      const int n = i / MT, j = i % MT;                                                      \
      acc[j][n] = mfma(cat8(wh[PAR][n][0], wh[PAR][n][1]), xf[PAR][j], acc[j][n]);           \
      if (DORD && i < 20 && i * NR / 20 != (i + 1) * NR / 20) read_one(PAR ^ 1, i * NR / 20, woff, xoff); \
      if (DBL && i % 3 == 2) {     \
        const int V = 2 * (PAR) - 2 + (i / 12), d = (i / 3) & 3, pc_ = V + 5;                \
        if (d < C::WI) dma_w(d, V + 4);                                                      \
        else {                                                                               \
          const int xi = ((pc_ & 1) ? C::XE : 0) + (d - C::WI);                              \
          if (xi < ((pc_ & 1) ? XI : C::XE)) dma_x(xi, pc_ >> 1, (pc_ >> 1) % 3);            \
        }                                                                                    \
      }                                                                                      \
      if (!DBL && DOW && i % 6 == 2) {                                                       \
        const int d = i / 6;                                                                 \
        if (d < C::WI) dma_w(d, (T) + 4);                                                    \
        else if (DOX) {                                                                      \
          const int xi = (PAR ? 0 : C::XE) + (d - C::WI);                                    \
          if (xi < (PAR ? C::XE : XI)) dma_x(xi, ((T) + 5) >> 1, PAR ? xr : xq);             \
        }                                                                                    \
      }                                                                                      \
      JOB_HOOK                                                                               \
      if (i == 21) st0 = mfma(xf[PAR][0], xf[PAR][0], st0);                                  \
      if (i == 22) {                                                                         \
        if (ROLE == 0) st1 = mfma(onesf, xf[PAR][0], st1);                                   \
        else if (wn == PAR) st1 = mfma(xf[PAR][1], xf[PAR][1], st1);                         \
      }                                                                                      \
      SB0;                                                                                   \
    }                                                                                        \
    if (PAR) xr = xn;                                                                        \
  }
  constexpr int VMF = n_issue<ROLE>(0) + n_issue<ROLE>(1);
  ITER(0, 0, 0, true, true, true, true)
  ITER(1, 1, n_issue<ROLE>(-1), true, true, true, true)
  for (int t = 2; t < 14; t += 2) {
    ITER(t, 0, VMF, true, true, true, false)
    ITER(t + 1, 1, VMF, true, true, true, false)
  }
  ITER(14, 0, VMF, true, true, true, false)
  ITER(15, 1, VMF, true, false, true, false)
  ITER(16, 0, n_issue<ROLE>(14) + n_issue<ROLE>(15), false, false, true, false)
  ITER(17, 1, n_issue<ROLE>(15), false, false, true, false)
  unsigned jl[8];
  const int J = job + w;
  const char* jsrc = job_src(wo, wd, J);
#undef JOB_HOOK
#define JOB_HOOK if (ROLE == 0 && i < 8) jl[i] = *(const unsigned*)(jsrc + (unsigned)((8 * g + i) * 1280 + m * 4));
  ITER(18, 0, 0, false, false, true, false)
#undef JOB_HOOK
#define JOB_HOOK
  ITER(19, 1, 0, false, false, false, false)
#undef ITER
#undef JOB_HOOK
  STAMP(2);

  LDS_AS float* rsum = (LDS_AS float*)(uintptr_t)(lds0 + STAT_OFF);
  LDS_AS float* rsq = rsum + 192;
  LDS_AS float* csum = rsum + 448;
  {
    const bool diag = g == q;
    const unsigned e0 = p == 0 ? ~0u : 0u, e1 = p == 1 ? ~0u : 0u, e2 = p == 2 ? ~0u : 0u, e3 = p == 3 ? ~0u : 0u;
    const float d0 = fbits((ubits(st0[0]) & e0) | (ubits(st0[1]) & e1) | (ubits(st0[2]) & e2) | (ubits(st0[3]) & e3));
    if (ROLE == 0) {
      if (diag) { rsq[lrow[0]] = d0; rsum[lrow[0]] = st1[0]; }
    } else {
      const float d1 = fbits((ubits(st1[0]) & e0) | (ubits(st1[1]) & e1) | (ubits(st1[2]) & e2) | (ubits(st1[3]) & e3));
      if (diag) {
        rsq[lrow[0]] = d0;
        rsq[wn ? 192 + wm * 16 + m : lrow[1]] = d1;
        if (wn) {
#pragma unroll
          for (int j = 0; j < MT; ++j) rsum[lrow[j]] = acc[j][7][0];
        }
      }
    }
  }
  if (wm == 0) {
#pragma unroll
    for (int j = 0; j < MT; ++j) {
      if (lrow[j] == 0) {
#pragma unroll
        for (int n = 0; n < NT; ++n) *(LDS_AS f32x4*)(csum + (wn * NT + n) * 16 + 4 * g) = acc[j][n];
      }
    }
  }
  lds_barrier();
  float ra[MT], rbv[MT], ir2[MT], k1[MT], k3[MT];
#pragma unroll
  for (int j = 0; j < MT; ++j) {
    const float mean = rsum[lrow[j]] * (1.f / D_);
    float sq = rsq[lrow[j]];
    if (ROLE == 1 && j == 1) sq += rsq[192 + wm * 16 + m];
    const float var = fmaxf(sq * (1.f / D_) - mean * mean, 0.f);
    ra[j] = __builtin_amdgcn_rsqf(var + 1e-5f);
    rbv[j] = ROLE == 0 ? -mean : -mean * ra[j];
    ir2[j] = var + 1e-5f;
    k1[j] = -2.3022082f * ra[j];
    k3[j] = -0.10294324f * ra[j] * ra[j] * ra[j];
  }
  STAMP(3);
  if (ROLE == 0) job_store(B2, J, true, jl, g, m);
  const rsrc_t a2r = mk_rsrc(A2, (unsigned)A2_BYTES);
  if (ROLE == 0) {
    unsigned so[2];
#pragma unroll
    for (int jp = 0; jp < 2; ++jp) {
      const int lr = (wm * 4 + ((2 * jp + (g & 1) + wn) & 3)) * 16 + m;
      const int gr = rbase + lr;
      so[jp] = (lr >= 1 && gr < S_) ? a2_off(gr, 640 + cg * 192 + wn * 16 + (g >> 1) * 8) : OOB;
    }
#pragma unroll
    for (int a = 0; a < 3; ++a) {
      const f32x4 cg4 = *(LDS_AS f32x4*)(csum + (wn * NT + 2 * a) * 16 + 4 * g);
      const f32x4 cu4 = *(LDS_AS f32x4*)(csum + (wn * NT + 2 * a + 1) * 16 + 4 * g);
#pragma unroll
      for (int jp = 0; jp < 2; ++jp) {
        unsigned hp[2][2];
#pragma unroll
        for (int jj = 0; jj < 2; ++jj) {
          const int j = 2 * jp + jj;
#pragma unroll
          for (int r = 0; r < 2; ++r) {
            const f32x2 ag = {acc[j][2 * a][2 * r], acc[j][2 * a][2 * r + 1]};
            const f32x2 au = {acc[j][2 * a + 1][2 * r], acc[j][2 * a + 1][2 * r + 1]};
            const f32x2 cgv = {cg4[2 * r], cg4[2 * r + 1]}, cuv = {cu4[2 * r], cu4[2 * r + 1]};
            const f32x2 tg = cgv * rbv[j] + ag;
            const f32x2 tu = cuv * rbv[j] + au;
            const f32x2 z = tg * (tg * tg * k3[j] + k1[j]);
            const f32x2 gu = tg * tu;
            const f32x2 ev = {__builtin_amdgcn_exp2f(z[0]), __builtin_amdgcn_exp2f(z[1])};
            const f32x2 dn = ev * ir2[j] + ir2[j];
            const float h0 = gu[0] * __builtin_amdgcn_rcpf(dn[0]);
            const float h1 = gu[1] * __builtin_amdgcn_rcpf(dn[1]);
            hp[jj][r] = pk2(h0, h1);
          }
        }
        auto s0 = __builtin_amdgcn_permlane16_swap(hp[0][0], hp[1][0], false, false);
        auto s1 = __builtin_amdgcn_permlane16_swap(hp[0][1], hp[1][1], false, false);
        bst128(a2r, so[jp] + a * 8192, (u32x4){s0[0], s1[0], s0[1], s1[1]});
      }
    }
  } else {
#pragma unroll
    for (int n = 0; n < NT; ++n) {
      const int tau = (n >> 2) * 8 + wn * 4 + (n & 3);
      const f32x4 c4 = *(LDS_AS f32x4*)(csum + (wn * NT + n) * 16 + 4 * g);
      if (tau < 15) {
#pragma unroll
        for (int j = 0; j < MT; ++j) {
          const f32x4 v = acc[j][n] * ra[j] + c4 * rbv[j];
          *(LDS_AS u32x2*)(uintptr_t)(lds0 + lrow[j] * QRS + tau * 32 + g * 8) = (u32x2){pk2(v[0], v[1]), pk2(v[2], v[3])};
        }
      }
    }
    lds_barrier();
    STAMP(4);
    const float sc = 0.22360679775f * 1.44269504089f;
    constexpr int NI = 6;
    const int h = w & 3, mt0 = 1 + (w >> 2);
    const bool odd = m & 1;
    const bool lea = 4 * g + (m & 1) <= m, leb = 4 * g + 2 + (m & 1) <= m;
    const unsigned mpar = odd ? 0xffff0000u : 0x0000ffffu;
    unsigned m0a = lea ? 0u : mpar, m1a = lea ? mpar : 0u, m0b = leb ? 0u : mpar, m1b = leb ? mpar : 0u;
    unsigned qma = g == 3 ? 0u : ~0u, qmb = g >= 2 ? 0u : ~0u;
    asm volatile("" : "+v"(m0a), "+v"(m1a), "+v"(m0b), "+v"(m1b), "+v"(qma), "+v"(qmb));
    const unsigned rq = lds0 + ((mt0 - 1) * 16 + m) * QRS + h * 40 + g * 16;
    const unsigned va = lds0 + ((mt0 - 1) * 16 + 4 * g + q) * QRS + 160 + h * 40 + p * 8;
    unsigned rq2 = rq + 96 * QRS, va2 = va + 96 * QRS;
    asm volatile("" : "+v"(rq2), "+v"(va2));
    const int kq = cg * 80 + h * 20;
    const unsigned vo0 = a2_off(m, kq + 4 * g), vo1 = g == 0 ? a2_off(m, kq + 16) : OOB;
    bf8 v0[NI], v1[NI];
    f32x4 s0[NI], s1[NI];
    const f32x4 z4 = {0.f, 0.f, 0.f, 0.f};
#pragma unroll
    for (int ii = 0; ii < NI; ++ii) {
      const unsigned r0 = (ii < 3 ? rq : rq2) + (ii % 3) * 32 * QRS, vv = (ii < 3 ? va : va2) + (ii % 3) * 32 * QRS;
      const u32x2 k0a = lds_b64(r0), k0b = lds_b64(r0 + 8);
      const u32x2 k1a = lds_b64(r0 + 16 * QRS), k1b = lds_b64(r0 + 16 * QRS + 8);
      const u32x2 qa = lds_b64(r0 + 16 * QRS + 320), qb = lds_b64(r0 + 16 * QRS + 328);
      v0[ii] = cat8(tr4(vv), tr4(vv + 16 * QRS));
      v1[ii] = cat8(tr4(vv + 32), tr4(vv + 16 * QRS + 32));
      const bf8 qf = __builtin_bit_cast(bf8, (u32x4){qa[0] & qma, qa[1] & qma, qb[0] & qmb, qb[1] & qmb});
      s0[ii] = mfma(__builtin_bit_cast(bf8, (u32x4){k0a[0], k0a[1], k0b[0], k0b[1]}), qf, z4);
      s1[ii] = mfma(__builtin_bit_cast(bf8, (u32x4){k1a[0], k1a[1], k1b[0], k1b[1]}), qf, z4);
    }
    u32x2 oa[NI], ob[NI];
    const unsigned ev = odd ? 0u : ~0u, od = ~ev, sa = lea ? ~0u : 0u, sb = leb ? ~0u : 0u;
    const bool bad = rbase + (mt0 - 1) * 16 < 0;
    const unsigned bada = (bad && !lea) ? ~0u : 0u, badb = (bad && !leb) ? ~0u : 0u;
    unsigned k00 = ev & ~sa, k01 = od & ~sa, k10 = ev & sa, k11 = od & sa;
    unsigned k02 = ev & ~sb, k03 = od & ~sb, k12 = ev & sb, k13 = od & sb;
    asm volatile("" : "+v"(k00), "+v"(k01), "+v"(k10), "+v"(k11), "+v"(k02), "+v"(k03), "+v"(k12), "+v"(k13));
    float la[NI], lb[NI], mx[NI];
#pragma unroll
    for (int ii = 0; ii < NI; ++ii) {
      unsigned ua = (ubits(s0[ii][0]) & k00) | (ubits(s0[ii][1]) & k01) | (ubits(s1[ii][0]) & k10) | (ubits(s1[ii][1]) & k11);
      unsigned ub = (ubits(s0[ii][2]) & k02) | (ubits(s0[ii][3]) & k03) | (ubits(s1[ii][2]) & k12) | (ubits(s1[ii][3]) & k13);
      if (ii == 0) {
        ua = (ua & ~bada) | (0xff7fffffu & bada);
        ub = (ub & ~badb) | (0xff7fffffu & badb);
      }
      la[ii] = fbits(ua); lb[ii] = fbits(ub);
      mx[ii] = vmax(la[ii], lb[ii]);
    }
#pragma unroll
    for (int ii = 0; ii < NI; ++ii) mx[ii] = red_max(mx[ii]) * sc;
#pragma unroll
    for (int ii = 0; ii < NI; ++ii) {
      la[ii] = __builtin_amdgcn_exp2f(la[ii] * sc - mx[ii]);
      lb[ii] = __builtin_amdgcn_exp2f(lb[ii] * sc - mx[ii]);
      mx[ii] = la[ii] + lb[ii];
    }
#pragma unroll
    for (int ii = 0; ii < NI; ++ii) mx[ii] = __builtin_amdgcn_rcpf(red_sum(mx[ii]));
#pragma unroll
    for (int ii = 0; ii < NI; ++ii) {
      const float pa_ = la[ii] * mx[ii], pb_ = lb[ii] * mx[ii];
      const unsigned pa = pk2(pa_, pa_), pb = pk2(pb_, pb_);
      const bf8 pf = __builtin_bit_cast(bf8, (u32x4){pa & m0a, pb & m0b, pa & m1a, pb & m1b});
      s0[ii] = mfma(v0[ii], pf, z4);
      s1[ii] = mfma(v1[ii], pf, z4);
    }
#pragma unroll
    for (int ii = 0; ii < NI; ++ii) {
      oa[ii] = (u32x2){pk2(s0[ii][0], s0[ii][1]), pk2(s0[ii][2], s0[ii][3])};
      ob[ii] = (u32x2){pk2(s1[ii][0], s1[ii][1]), pk2(s1[ii][2], s1[ii][3])};
    }
#pragma unroll
    for (int ii = 0; ii < NI; ++ii) {
      const int mt = mt0 + 2 * ii, T = rb * 11 - 1 + mt;
      if (mt < 12 && T < 128) {
        const int so = (T >> 2) * (KS2 * 4096) + (T & 3) * 1024;
        __builtin_amdgcn_raw_buffer_store_b64(oa[ii], a2r, vo0, so, ST_AUX);
        __builtin_amdgcn_raw_buffer_store_b64(ob[ii], a2r, vo1, so, ST_AUX);
      }
    }
  }
  STAMP(5);
}

__global__ __launch_bounds__(512, 2) void phase1(const char* __restrict__ x, const char* __restrict__ wqkv,
                                                 const char* __restrict__ wo, const char* __restrict__ wg,
                                                 const char* __restrict__ wu, const char* __restrict__ wd,
                                                 char* __restrict__ A2, char* __restrict__ B2) {
  __shared__ __attribute__((aligned(1024))) char smem[LDS1_BYTES];
  const unsigned lds0 = (unsigned)(uintptr_t)(LDS_AS char*)smem;
  const int b = blockIdx.x, xcd = b & 7, slot = b >> 3;
  if (slot >= 12 && slot < 16) {
    unsigned long long pc0;
    int dlen;
    asm volatile("s_getpc_b64 %0\nP1_REF_LBL:\n\ts_mov_b32 %1, P2_TAIL_LBL-P1_REF_LBL" : "=s"(pc0), "=s"(dlen));
    const unsigned long long lo = pc0 & ~15ull;
    const unsigned len = dlen > 0 ? (unsigned)dlen : 0u;
    unsigned o = ((slot - 12) * 512 + threadIdx.x) * 16;
    unsigned sink = 0;
    for (; o + 16 <= len && o < 131072u; o += 32768) sink ^= (*(const u32x4*)((const char*)lo + o))[0];
    asm volatile("" ::"v"(sink));
  }
  if (slot < 12) {
    const int hg = 2 * (xcd & 3) + slot / 6, rb = 6 * (xcd >> 2) + slot % 6;
    phase1_body<1>(x, wqkv, wqkv, wo, wd, A2, B2, rb, hg, lds0, 0);
  } else {
    const int L = xcd * 20 + (slot - 12);
    if (L >= 153) {
      const int lane = threadIdx.x & 63, w = __builtin_amdgcn_readfirstlane(threadIdx.x >> 6), g = lane >> 4, m = lane & 15;
      unsigned jl[5][8];
#pragma unroll
      for (int r = 0; r < 5; ++r) {
        const int J = 1224 + r * 56 + (L - 153) * 8 + w;
        const char* jsrc = job_src(wo, wd, J < 1480 ? J : J - 1480);
#pragma unroll
        for (int i = 0; i < 8; ++i) jl[r][i] = *(const unsigned*)(jsrc + (unsigned)((8 * g + i) * 1280 + m * 4));
      }
#pragma unroll
      for (int r = 0; r < 5; ++r) {
        const int J = 1224 + r * 56 + (L - 153) * 8 + w;
        job_store(B2, J < 1480 ? J : J - 1480, J < 1480, jl[r], g, m);
      }
      return;
    }
    int rb, cg;
    if (L < 72) { cg = L >> 3; rb = L & 7; } else { cg = (L - 72) / 9; rb = 8 + (L - 72) % 9; }
    phase1_body<0>(x, wg, wu, wo, wd, A2, B2, rb, cg, lds0, L * 8);
  }
}

__global__ __launch_bounds__(256, 1) void phase2(const char* __restrict__ A2, const char* __restrict__ B2,
                                                 const char* __restrict__ x, char* __restrict__ out) {
  __shared__ __attribute__((aligned(16))) f32x4 red[4][3][5][64];
  const int tid = threadIdx.x, lane = tid & 63;
  const int w = __builtin_amdgcn_readfirstlane(tid >> 6);
  const int g = lane >> 4, m = lane & 15;
  const int b = blockIdx.x, xcd = b & 7, slot = b >> 3;
  const int rb = (xcd >> 1) * 8 + (slot & 7), cg = (xcd & 1) * 4 + (slot >> 3);
  const int pw = ((w & 1) << 1) | (w >> 1);
  const char* ap = A2 + (size_t)(rb * KS2) * 4096;
  const char* bp = B2 + (size_t)(cg * KS2) * 5120;
  const unsigned lo = lane * 16;
  unsigned la[4];
#pragma unroll
  for (int j = 0; j < 4; ++j) la[j] = lo + (j ^ pw) * 1024;
  STAMP2(0);
  constexpr int DEP = P2DEP, NI = 19;
  u32x4 af[DEP][4], bfr[DEP][5];
  f32x4 acc[4][5];
#pragma unroll
  for (int i = 0; i < 4; ++i)
#pragma unroll
    for (int n = 0; n < 5; ++n) acc[i][n] = (f32x4){0.f, 0.f, 0.f, 0.f};
  auto load1 = [&](int i, int u) {
    const int ks = i == NI - 1 ? 72 + (w >> 1) : w + 4 * i;
    if (u < 5) bfr[i % DEP][u] = *(const u32x4*)(bp + ks * 5120 + (lo + u * 1024));
    else if (i < NI - 1 || u < 7) af[i % DEP][u - 5] = *(const u32x4*)(ap + ks * 4096 + la[u - 5]);
  };
  const int orow = rb * 64 + pw * 16 + m;
  const size_t ooff = (size_t)orow * 1280 + (cg * 80 + 4 * g) * 2;
  u32x2 xr[5];
#pragma unroll
  for (int i = 0; i < DEP - 1; ++i)
#pragma unroll
    for (int u = 0; u < 9; ++u) load1(i, u);
#pragma unroll
  for (int n = 0; n < 5; ++n) xr[n] = *(const u32x2*)(x + ooff + n * 32);
  SB0;
#pragma unroll
  for (int i = 0; i < NI; ++i) {
#pragma unroll
    for (int u = 0; u < (i == NI - 1 ? 10 : 20); ++u) {
      const int j = u / 5, n = u % 5;
      acc[j][n] = mfma(__builtin_bit_cast(bf8, bfr[i % DEP][n]), __builtin_bit_cast(bf8, af[i % DEP][j]), acc[j][n]);
      if ((u & 1) == 0 && u < 18 && i + DEP - 1 < NI) load1(i + DEP - 1, u >> 1);
      SB0;
    }
    if (i == NI - 2) {
#pragma unroll
      for (int j = 2; j < 4; ++j)
#pragma unroll
        for (int n = 0; n < 5; ++n) red[j ^ pw][j - 1][n][lane] = acc[j][n];
    }
  }
  STAMP2(1);
#pragma unroll
  for (int n = 0; n < 5; ++n) red[1 ^ pw][0][n][lane] = acc[1][n];
  __syncthreads();
#pragma unroll
  for (int n = 0; n < 5; ++n) {
    const f32x4 s = (acc[0][n] + red[pw][0][n][lane]) + (red[pw][1][n][lane] + red[pw][2][n][lane]);
    const float x0 = fbits(xr[n][0] << 16), x1 = fbits(xr[n][0] & 0xffff0000u);
    const float x2 = fbits(xr[n][1] << 16), x3 = fbits(xr[n][1] & 0xffff0000u);
    *(u32x2*)(out + ooff + n * 32) = (u32x2){pk2(s[0] + x0, s[1] + x1), pk2(s[2] + x2, s[3] + x3)};
  }
  asm volatile("P2_TAIL_LBL:");
  STAMP2(2);
}

extern "C" void kernel_launch(void* const* d_in, const int* in_sizes, int n_in, void* d_out, int out_size, void* d_ws,
                              size_t ws_size, hipStream_t stream) {
  const char* x = (const char*)d_in[0];
  const char* wqkv = (const char*)d_in[1];
  const char* wo = (const char*)d_in[2];
  const char* wg = (const char*)d_in[4];
  const char* wu = (const char*)d_in[5];
  const char* wd = (const char*)d_in[6];
  char* A2 = (char*)d_ws;
  char* B2 = A2 + A2_BYTES;
  phase1<<<256, 512, 0, stream>>>(x, wqkv, wo, wg, wu, wd, A2, B2);
  phase2<<<256, 256, 0, stream>>>(A2, B2, x, (char*)d_out);
}
```
